```python
import torch

def _softmax(x, axis=-1):
    return torch.softmax(x, dim=axis)
def _gelu(x):
    return torch.nn.functional.gelu(x, approximate="tanh")
def _silu(x):
    return torch.nn.functional.silu(x)
def _sigmoid(x):
    return torch.sigmoid(x)
def _layernorm(x, axis=-1, eps=1e-5):
    m = x.mean(dim=axis, keepdim=True)
    v = x.var(dim=axis, keepdim=True, unbiased=False)
    return (x - m) / torch.sqrt(v + eps)
def _scatter_add(tgt, idx, src, axis=0):
    return tgt.clone().index_add_(axis, idx, src)
def _segment_sum(src, seg, n):
    out = torch.zeros((n,) + src.shape[1:], dtype=src.dtype)
    return out.index_add_(0, seg, src)
def _pad(x, pw):
    flat = tuple(v for pair in reversed(pw) for v in pair)
    return torch.nn.functional.pad(x, flat)

kernel_name = 'parallel_sum__ssm_conv_s5__dense_activation_gelu__prompt'

def setup_inputs():

    _w = (lambda a: (lambda t: t.to(torch.bfloat16) if t.is_floating_point() else t)(torch.as_tensor(a)))
    def _A_setup():
        B, S, D = 1, 2048, 512
        P = D
        Lambda_re = -0.5 * (lambda _x: torch.exp(torch.as_tensor(_x)))((lambda *s: torch.randn(*s, dtype=torch.bfloat16) * 0.2)(P))
        Lambda_im = (lambda *s: torch.randn(*s, dtype=torch.bfloat16) * 0.8)(P) * 1.0
        B_re = (lambda *s: torch.randn(*s, dtype=torch.bfloat16) * 0.4)(P, D) * (D ** -0.5)
        B_im = (lambda *s: torch.randn(*s, dtype=torch.bfloat16) * 0.4)(P, D) * (D ** -0.5)
        C_re = (lambda *s: torch.randn(*s, dtype=torch.bfloat16) * 0.4)(D, P) * (P ** -0.5)
        C_im = (lambda *s: torch.randn(*s, dtype=torch.bfloat16) * 0.4)(D, P) * (P ** -0.5)
        log_dt = (lambda *s: torch.randn(*s, dtype=torch.bfloat16) * 0.2)(P) * 0.3 - 2.0
        Dskip = (lambda *s: torch.randn(*s, dtype=torch.bfloat16) * 0.05)(D)
        return {
            "x": _w((lambda *s: torch.randn(*s, dtype=torch.bfloat16) * 0.05)(B, S, D)),
            "Lambda_re": _w(Lambda_re),
            "Lambda_im": _w(Lambda_im),
            "B_re": _w(B_re),
            "B_im": _w(B_im),
            "C_re": _w(C_re),
            "C_im": _w(C_im),
            "log_dt": _w(log_dt),
            "Dskip": _w(Dskip),
        }
    _A_in = _A_setup()

    def _B_setup():
        B, S, D, F = 1, 2048, 512, 1792
        return {
            "x": _w((lambda *s: torch.randn(*s, dtype=torch.bfloat16) * 0.05)(B, S, D)),
            "w1": _w((lambda *s: torch.randn(*s, dtype=torch.bfloat16) * 0.8)(D, F) * (D ** -0.5)),
            "b1": _w((lambda *s: torch.randn(*s, dtype=torch.bfloat16) * 12.8)(F) * 0.01),
            "w2": _w((lambda *s: torch.randn(*s, dtype=torch.bfloat16) * 0.8)(F, D) * (F ** -0.5)),
            "b2": _w((lambda *s: torch.randn(*s, dtype=torch.bfloat16) * 12.8)(D) * 0.01),
        }
    _B_in = _B_setup()

    return {
        "A_x": _A_in["x"],
        "A_Lambda_re": _A_in["Lambda_re"],
        "A_Lambda_im": _A_in["Lambda_im"],
        "A_B_re": _A_in["B_re"],
        "A_B_im": _A_in["B_im"],
        "A_C_re": _A_in["C_re"],
        "A_C_im": _A_in["C_im"],
        "A_log_dt": _A_in["log_dt"],
        "A_Dskip": _A_in["Dskip"],
        "B_x": _B_in["x"],
        "B_w1": _B_in["w1"],
        "B_b1": _B_in["b1"],
        "B_w2": _B_in["w2"],
        "B_b2": _B_in["b2"],
    }

def reference(A_x, A_Lambda_re, A_Lambda_im, A_B_re, A_B_im, A_C_re, A_C_im, A_log_dt, A_Dskip, B_x, B_w1, B_b1, B_w2, B_b2):
    def _A_ref(x, Lambda_re, Lambda_im, B_re, B_im, C_re, C_im, log_dt, Dskip):
        Bsz, L, H = x.shape
        Lam = Lambda_re + 1j * Lambda_im
        Bmat = B_re + 1j * B_im
        Cmat = C_re + 1j * C_im
        dt = torch.exp(log_dt)
        Lam_bar = torch.exp(dt * Lam)
        B_bar = ((Lam_bar - 1.0) / Lam)[:, None] * Bmat
        Bu = torch.einsum("bld,pd->blp", x + 0j, B_bar)
        h = torch.zeros_like(Bu[:, 0, :])
        hs = []
        for t in range(L):
            h = Lam_bar[None, :] * h + Bu[:, t, :]
            hs.append(h)
        hseq = torch.stack(hs, 1)
        y = 2.0 * torch.einsum("blp,dp->bld", hseq, Cmat).real + x * Dskip[None, None, :]
        return y

    def _B_ref(x, w1, b1, w2, b2):
        h = torch.einsum("bsd,df->bsf", x, w1) + b1
        h = _gelu(h)
        return torch.einsum("bsf,fd->bsd", h, w2) + b2

    x = A_x
    xn = _layernorm(x, axis=-1)
    return x + _A_ref(xn, A_Lambda_re, A_Lambda_im, A_B_re, A_B_im, A_C_re, A_C_im, A_log_dt, A_Dskip) + _B_ref(xn, B_w1, B_b1, B_w2, B_b2)
```

```cpp
#include <hip/hip_runtime.h>
#include <hip/hip_bf16.h>
typedef unsigned short u16;
__device__ inline float bf2f(u16 b){ return __uint_as_float(((unsigned)b)<<16); }
__device__ inline u16 f2bf(float f){ unsigned u=__float_as_uint(f); u += 0x7fffu + ((u>>16)&1u); return (u16)(u>>16); }
__device__ inline float rbf(float f){ return bf2f(f2bf(f)); }
constexpr int L=2048, D=512, P=512, F=1792;
__global__ void k_ln(const u16* x, float* xn){
  int row=blockIdx.x; int t=threadIdx.x;
  float v[8]; float s=0;
  for(int i=0;i<8;i++){ v[i]=bf2f(x[row*D+t*8+i]); s+=v[i]; }
  for(int o=32;o;o>>=1) s+=__shfl_xor(s,o);
  float m=s/D; float q=0;
  for(int i=0;i<8;i++){ float d=v[i]-m; q+=d*d; }
  for(int o=32;o;o>>=1) q+=__shfl_xor(q,o);
  float var=rbf(q/D); float mb=rbf(m);
  float sd=rbf(sqrtf(rbf(var+1e-5f)));
  for(int i=0;i<8;i++) xn[row*D+t*8+i]=rbf(rbf(v[i]-mb)/sd);
}
__global__ void k_g1(const float* xn,const u16* Bre,const u16* Bim,const u16* w1,const u16* b1,float* H){
  int n=blockIdx.x*256+threadIdx.x; int row=blockIdx.y;
  __shared__ float xs[D];
  for(int i=threadIdx.x;i<D;i+=256) xs[i]=xn[row*D+i];
  __syncthreads();
  float a=0;
  if(n<512){ for(int k=0;k<D;k++) a+=xs[k]*bf2f(Bre[n*D+k]); }
  else if(n<1024){ for(int k=0;k<D;k++) a+=xs[k]*bf2f(Bim[(n-512)*D+k]); }
  else { int f=n-1024; for(int k=0;k<D;k++) a+=xs[k]*bf2f(w1[k*F+f]); a+=bf2f(b1[f]);
    float u=0.7978845608028654f*(a+0.044715f*a*a*a); a=0.5f*a*(1.f+tanhf(u)); }
  H[row*2816+n]=a;
}
__global__ void k_scan(float* H,const u16* Lre,const u16* Lim,const u16* ldt){
  int p=blockIdx.x*64+threadIdx.x;
  float lr=bf2f(Lre[p]), li=bf2f(Lim[p]); float dt=rbf(expf(bf2f(ldt[p])));
  float er=expf(dt*lr); float br=er*cosf(dt*li), bi=er*sinf(dt*li);
  float nr=br-1.f, ni=bi; float den=lr*lr+li*li;
  float cr=(nr*lr+ni*li)/den, ci=(ni*lr-nr*li)/den;
  float hr=0,hi=0;
  for(int t=0;t<L;t++){
    float ur=H[t*2816+p], ui=H[t*2816+512+p];
    float vr=cr*ur-ci*ui, vi=cr*ui+ci*ur;
    float tr=br*hr-bi*hi+vr, ti=br*hi+bi*hr+vi; hr=tr; hi=ti;
    H[t*2816+p]=hr; H[t*2816+512+p]=hi;
  }
}
__global__ void k_g2(const float* H,const float* xn,const u16* x,const u16* Cre,const u16* Cim,const u16* w2,const u16* b2,const u16* Ds,float* out){
  int d=blockIdx.x*256+threadIdx.x; int row=blockIdx.y;
  __shared__ float hs[2816];
  for(int i=threadIdx.x;i<2816;i+=256) hs[i]=H[row*2816+i];
  __syncthreads();
  float a=0,b=0;
  for(int k=0;k<P;k++) a+=hs[k]*bf2f(Cre[d*P+k])-hs[512+k]*bf2f(Cim[d*P+k]);
  for(int k=0;k<F;k++) b+=hs[1024+k]*bf2f(w2[k*D+d]);
  out[row*D+d]=bf2f(x[row*D+d])+2.f*a+xn[row*D+d]*bf2f(Ds[d])+b+bf2f(b2[d]);
}
extern "C" void kernel_launch(void* const* d_in, const int* in_sizes, int n_in,
                              void* d_out, int out_size, void* d_ws, size_t ws_size,
                              hipStream_t stream) {
  const u16* x=(const u16*)d_in[0];
  float* xn=(float*)d_ws; float* H=xn+L*D;
  k_ln<<<L,64,0,stream>>>(x,xn);
  k_g1<<<dim3(11,L),256,0,stream>>>(xn,(const u16*)d_in[3],(const u16*)d_in[4],(const u16*)d_in[10],(const u16*)d_in[11],H);
  k_scan<<<8,64,0,stream>>>(H,(const u16*)d_in[1],(const u16*)d_in[2],(const u16*)d_in[7]);
  k_g2<<<dim3(2,L),256,0,stream>>>(H,xn,x,(const u16*)d_in[5],(const u16*)d_in[6],(const u16*)d_in[12],(const u16*)d_in[13],(const u16*)d_in[8],(float*)d_out);
}
```

```cpp
#include <hip/hip_runtime.h>
#include <hip/hip_bf16.h>

typedef unsigned short u16;
typedef unsigned u32;
typedef unsigned long long u64;
typedef short bf16x8 __attribute__((ext_vector_type(8)));
typedef short bf16x4 __attribute__((ext_vector_type(4)));
typedef float f32x16 __attribute__((ext_vector_type(16)));
typedef float f32x4 __attribute__((ext_vector_type(4)));
typedef float f32x2 __attribute__((ext_vector_type(2)));
typedef __bf16 bf16x2n __attribute__((ext_vector_type(2)));
typedef u32 u32x4 __attribute__((ext_vector_type(4)));
typedef u32 u32x2 __attribute__((ext_vector_type(2)));
typedef int i32x4 __attribute__((ext_vector_type(4)));
#define LAS __attribute__((address_space(3)))
#define DI __device__ __forceinline__
#define AI __attribute__((always_inline))
#define SB __builtin_amdgcn_sched_barrier(0)
#define PRIO(n) do { SB; asm volatile("s_setprio " #n); SB; } while (0)
#define MFMA32(a, b, c) __builtin_amdgcn_mfma_f32_32x32x16_bf16((a), (b), (c), 0, 0, 0)
#define BC(T, v) __builtin_bit_cast(T, v)
#define SC1 16

constexpr u32 WS_FF = 4096;
constexpr u32 WS_FI = WS_FF + 256 * 2560;
constexpr u32 WS_GR = WS_FI + 131072;
constexpr u32 WS_GS = WS_GR + 131072;
constexpr u32 WS_GB = WS_GS + 16384;
constexpr u32 WS_GW = WS_GB + 8192;
constexpr u32 WS_HF = 1310720;
constexpr u32 WS_HS = WS_HF + 16 * 4 * 56 * 2048;
constexpr u32 WS_X = WS_HS + 16 * 4 * 64 * 1024;
constexpr u32 WS_END = WS_X + 16 * 4 * 4 * 4 * 6144;
static_assert(WS_GW + 8 * 1792 * 8 <= WS_HF, "ws map");

constexpr int WB = 49152;
constexpr int RO[3] = {16384, 0, 32768};
constexpr int U1S[8] = {12, 13, 14, 15, 10, 11, 8, 9};
constexpr int W2S[14] = {16, 0, 1, 2, 3, 12, 13, 14, 15, 16, 0, 1, 2, 3};
constexpr int MI_TR = 139264, MI_PW = MI_TR, MI = MI_TR + 8 * 2304;
constexpr int MI_PS = MI, MI_CT = MI + 128, MI_RC = MI_CT + 1024, MI_RS = MI_RC + 2048, MI_NM = MI_RS + 512, MI_WT = MI_NM + 512, MI_HP = MI_WT + 1024,
              MI_CNT = MI_HP, MI_JUNK = MI_CNT + 128;
constexpr int MI_HQ = MI_CT;
constexpr int LDS_BYTES = 163840;
static_assert(MI_JUNK + 256 + 128 <= LDS_BYTES, "lds map");
constexpr int C_WT = 0, C_HP = 4, C_U1 = 8, C_LA = 12, C_LB = 16, C_LC = 20, C_LD = 24, C_P1 = 28, C_P2 = 32, C_P3 = 36, C_PAIR = 64;
constexpr int OOB = 0x40000000;
constexpr u32 SPIN_CAP = 1u << 18;
#define KD 22

#define STAMP(i)
#define SET 0
#define STS(set, i) do { if (SET == (set)) STAMP(i); } while (0)

DI float asf(u32 b) { return BC(float, b); }
DI u32 asu(float f) { return BC(u32, f); }
DI float bf2f(u32 b) { return asf(b << 16); }
DI float bfh(u32 b) { return asf(b & 0xffff0000u); }
DI u32 pk2(float a, float b) {
  f32x2 v = {a, b};
  return BC(u32, __builtin_convertvector(v, bf16x2n));
}
DI float rbf(float a) { return bf2f(pk2(a, a)); }
DI i32x4 mkrs(u64 a, u32 bytes) {
  i32x4 r = {(int)(u32)a, (int)((u32)(a >> 32) & 0xffffu), (int)bytes, 0x00020000};
  return r;
}
template <int SI, int LI> DI void dma1(i32x4 rs, int v, int sb, int lw) {
  int t;
  asm volatile("s_add_i32 m0, %[lw], %[li]\n\ts_add_i32 %[t], %[sb], %[si]\n\tbuffer_load_dwordx4 %[v], %[rs], %[t] offen lds"
               : [t] "=&s"(t)
               : [v] "v"(v), [rs] "s"(rs), [sb] "s"(sb), [lw] "s"(lw), [si] "n"(SI), [li] "n"(LI)
               : "memory", "scc");
}
template <int LI> DI void dma_s(i32x4 rs, int v, int& so, int ps, int lw) {
  asm volatile("s_add_i32 m0, %[lw], %[li]\n\ts_add_i32 %[t], %[t], %[ps]\n\tbuffer_load_dwordx4 %[v], %[rs], %[t] offen lds"
               : [t] "+s"(so)
               : [v] "v"(v), [rs] "s"(rs), [ps] "s"(ps), [lw] "s"(lw), [li] "n"(LI)
               : "memory", "scc");
}
DI void touch(const void* p, int lw) {
  asm volatile("s_mov_b32 m0, %1\n\ts_nop 0\n\tglobal_load_lds_dword %0, off" ::"v"(p), "s"(lw) : "memory");
}
template <int I> struct IC { static constexpr int v = I; };
template <int A, int B, class Fn> DI void sfor(Fn&& f) {
  if constexpr (A < B) {
    f(IC<A>{});
    sfor<A + 1, B>(f);
  }
}
typedef f32x2 cf;
template <class T> DI void pin1(T& v) { asm volatile("" : "+v"(v)); }
template <class... T> DI void pin(T&... v) { (pin1(v), ...); }
DI int opq(int v) { asm volatile("" : "+v"(v)); return v; }
DI int opqs(int v) { asm volatile("" : "+s"(v)); return v; }
template <int N> DI void bar() { asm volatile("s_waitcnt vmcnt(%0) lgkmcnt(0)\n\ts_barrier" ::"n"(N) : "memory"); }
template <int N> DI void waitv() { asm volatile("s_waitcnt vmcnt(%0)" ::"n"(N) : "memory"); }
DI void waitl() { asm volatile("s_waitcnt lgkmcnt(0)" ::: "memory"); }
DI bf16x8 ld128(int a) { return *(LAS bf16x8*)(size_t)a; }
DI bf16x8 ldtr(int a, int d) {
  bf16x4 lo = __builtin_amdgcn_ds_read_tr16_b64_v4i16((LAS bf16x4*)(size_t)a);
  bf16x4 hi = __builtin_amdgcn_ds_read_tr16_b64_v4i16((LAS bf16x4*)(size_t)(a + d));
  return __builtin_shufflevector(lo, hi, 0, 1, 2, 3, 4, 5, 6, 7);
}
#define LDS(T, a) (*(LAS T*)(size_t)(a))
DI float add32(float a, float b) {
  auto r = __builtin_amdgcn_permlane32_swap(asu(a), asu(b), false, false);
  return asf(r[0]) + asf(r[1]);
}
DI float add16(float a, float b) {
  auto r = __builtin_amdgcn_permlane16_swap(asu(a), asu(b), false, false);
  return asf(r[0]) + asf(r[1]);
}
template <int C> DI float dppadd(float v) { return v + asf((u32)__builtin_amdgcn_update_dpp(0, (int)asu(v), C, 0xf, 0xf, true)); }
DI float rsum(float v) {
  v = dppadd<0xB1>(v);
  v = dppadd<0x4E>(v);
  v = dppadd<0x141>(v);
  return dppadd<0x140>(v);
}
DI void halves(float v, float& a, float& b) {
  auto r = __builtin_amdgcn_permlane32_swap(asu(v), asu(v), false, false);
  a = asf(r[0]);
  b = asf(r[1]);
}
DI u32 split2(float c) {
  return pk2(c, c - rbf(c));
}
DI float dot2(u32 a, u32 b, float c) { return __builtin_amdgcn_fdot2_f32_bf16(BC(bf16x2n, a), BC(bf16x2n, b), c, false); }
DI cf pfma(cf a, cf b, cf c) {
  asm("v_pk_fma_f32 %0, %1, %2, %0 op_sel_hi:[0,1,1]\n\tv_pk_fma_f32 %0, %1, %2, %0 op_sel:[1,1,0] op_sel_hi:[1,0,1] neg_lo:[1,0,0]"
      : "+&v"(c)
      : "v"(a), "v"(b));
  return c;
}
DI cf pmul(cf a, cf b) {
  cf t;
  asm("v_pk_mul_f32 %0, %1, %2 op_sel_hi:[0,1]\n\tv_pk_fma_f32 %0, %1, %2, %0 op_sel:[1,1,0] op_sel_hi:[1,0,1] neg_lo:[1,0,0]"
      : "=&v"(t)
      : "v"(a), "v"(b));
  return t;
}
DI cf csq(cf a) { return pmul(a, a); }
DI float sfma(float a, float b, float c) { asm("v_fma_f32 %0, %1, %2, %3" : "=v"(c) : "v"(a), "v"(b), "v"(c)); return c; }
DI cf cfma(cf a, cf b, cf c) {
  float x = c.x, y = c.y;
  asm("v_fmac_f32 %0, %2, %4\n\tv_fmac_f32 %1, %2, %5\n\tv_fma_f32 %0, -%3, %5, %0\n\tv_fmac_f32 %1, %3, %4"
      : "+&v"(x), "+&v"(y)
      : "v"(a.x), "v"(a.y), "v"(b.x), "v"(b.y));
  return cf{x, y};
}
DI cf cmul(cf a, cf b) {
  float x, y;
  asm("v_mul_f32 %0, %2, %4\n\tv_mul_f32 %1, %2, %5\n\tv_fma_f32 %0, -%3, %5, %0\n\tv_fmac_f32 %1, %3, %4"
      : "=&v"(x), "=&v"(y)
      : "v"(a.x), "v"(a.y), "v"(b.x), "v"(b.y));
  return cf{x, y};
}
DI cf csel(bool c, cf a, cf b) { return cf{c ? a.x : b.x, c ? a.y : b.y}; }
DI void settle(cf& a) { asm volatile("s_nop 1" : "+v"(a)); }
template <int O> DI void sig(int sv) { asm volatile("ds_inc_u32 %0, %0 offset:%1" ::"v"(sv), "n"(O) : "memory"); }
template <int O> DI u32 peek(int sv) { return *(volatile LAS u32*)(size_t)(sv + O); }
template <int O, int N> DI void chk(int sv, u32 v) {
  int t, c;
  asm volatile("v_readfirstlane_b32 %[t], %[v]\n\ts_cmp_ge_u32 %[t], %[n]\n\ts_cbranch_scc1 2f\n\ts_mov_b32 %[c], 0x400000\n"
               "1:\n\ts_sleep 1\n\tds_read_b32 %[v], %[a] offset:%[o]\n\ts_sub_u32 %[c], %[c], 1\n\ts_waitcnt lgkmcnt(0)\n\ts_cbranch_scc1 2f\n\t"
               "v_readfirstlane_b32 %[t], %[v]\n\ts_cmp_lt_u32 %[t], %[n]\n\ts_cbranch_scc1 1b\n2:"
               : [t] "=&s"(t), [c] "=&s"(c), [v] "+v"(v)
               : [a] "v"(sv), [o] "n"(O), [n] "n"(N)
               : "memory", "scc");
}
template <int O, int N> DI void lwait(int sv) { chk<O, N>(sv, peek<O>(sv)); }
DI void ldg4(u32x4& d, int vo, i32x4 rs, int so) {
  asm volatile("buffer_load_dwordx4 %0, %1, %2, %3 offen sc1" : "=v"(d) : "v"(vo), "s"(rs), "s"(so) : "memory");
}
template <int O> DI void ldg4o(u32x4& d, int vo, i32x4 rs, int so) {
  asm volatile("buffer_load_dwordx4 %0, %1, %2, %3 offen offset:%4 sc1" : "=v"(d) : "v"(vo), "s"(rs), "s"(so), "n"(O) : "memory");
}
DI void ldg2(u32x2& d, int vo, i32x4 rs) { asm volatile("buffer_load_dwordx2 %0, %1, %2, 0 offen sc1" : "=v"(d) : "v"(vo), "s"(rs) : "memory"); }
template <int O> DI void stg4(u32x4 d, int vo, i32x4 rs, int so) {
  asm volatile("buffer_store_dwordx4 %0, %1, %2, %3 offen offset:%4 sc1" ::"v"(d), "v"(vo), "s"(rs), "s"(so), "n"(O) : "memory");
}
template <int O> DI void stg2(u32x2 d, int vo, i32x4 rs) {
  asm volatile("buffer_store_dwordx2 %0, %1, %2, 0 offen offset:%3 sc1" ::"v"(d), "v"(vo), "s"(rs), "n"(O) : "memory");
}

constexpr int aextra(int n) { return n == 17 ? 4 : ((n == 1 || n == 21 || n == 23 || (n >= 6 && n <= 16 && !(n & 1))) ? 1 : 0); }
constexpr int abar(int nb) {
  int c = (nb >= 6 && nb != 22) ? aextra(nb - 5) : 0;
  for (int m = nb < 6 ? 0 : nb - 4; m < nb; ++m) c += (m < 2 ? 2 : 1) + aextra(m);
  return c;
}
#define KS 6
#define KT 4
#define GA0 0
#define GA1 56
#define GB0 66
#define GB1 101
#define DF 16
#define LD0 24
#define LD1 24
#define LD2 26
#define LD3 26
#define LD4 18
constexpr int gapof(int k, int i) { return 64 + (k < KD ? 4 * k + i : 4 * KD + 3 * (k - KD) + i - 1); }
constexpr int gap1(int k) { return gapof(k, k < KD ? 0 : 1); }
constexpr int gsub(int g, int g0, int g1) { return g <= g0 ? 0 : (g >= g1 ? 66 : ((g - g0) * 66 + (g1 - g0) - 1) / (g1 - g0)); }
constexpr int gdone(int t, int g0, int g1) { int g = g0; while (gsub(g + 1, g0, g1) <= t) ++g; return g; }
enum { E_ST, E_SS, E_FL, E_LD };
struct Ev { int gap, kind, arg; };
struct EvTab { Ev e[64]; int n; };
constexpr int GK[6] = {10, 16, 22, 34, 40, 42};
constexpr int LDA[5] = {LD0, LD1, LD2, LD3, LD4};
constexpr int chkgap(int g) { return gap1(GK[g]) - LDA[g]; }
constexpr EvTab mkev() {
  EvTab t{};
  int sg[8] = {};
  for (int f = 0; f < 8; ++f) {
    sg[f] = f < 4 ? gdone(16 * f + 17, GA0, GA1) : gdone(16 * (f - 4) + 17, GB0, GB1);
    t.e[t.n++] = Ev{sg[f], E_ST, f};
  }
  for (int f = 0; f < 4; ++f) t.e[t.n++] = Ev{gap1(KS) + 2 * f, E_SS, f};
  t.e[t.n++] = Ev{sg[1] + DF, E_FL, 0};
  t.e[t.n++] = Ev{sg[3] + DF, E_FL, 1};
  t.e[t.n++] = Ev{gap1(KS) + 6 + DF, E_FL, 2};
  t.e[t.n++] = Ev{sg[5] + DF, E_FL, 3};
  t.e[t.n++] = Ev{sg[7] + DF, E_FL, 4};
  for (int g = 0; g < 5; ++g)
    for (int k = GK[g]; k < GK[g + 1]; ++k) t.e[t.n++] = Ev{chkgap(g) + 2 * (k - GK[g]), E_LD, k};
  for (int a = 1; a < t.n; ++a)
    for (int c = a; c > 0 && t.e[c - 1].gap > t.e[c].gap; --c) {
      const Ev x = t.e[c];
      t.e[c] = t.e[c - 1];
      t.e[c - 1] = x;
    }
  return t;
}
constexpr EvTab EV = mkev();
constexpr int evfirst(int g) { int a = 0; while (a < EV.n && EV.e[a].gap < g) ++a; return a; }
constexpr int evfind(int kind, int arg) { int a = 0; while (EV.e[a].kind != kind || EV.e[a].arg != arg) ++a; return a; }
constexpr int flaglast(int g) { return g == 2 ? evfind(E_SS, 3) : evfind(E_ST, g < 2 ? 2 * g + 1 : 2 * g - 1); }
static_assert(gdone(33, GA0, GA1) < gap1(1) && gdone(65, GA0, GA1) < gap1(3) && GA1 <= GB0, "first unit");
static_assert(gdone(17, GB0, GB1) < gap1(KT) && gdone(33, GB0, GB1) < gap1(KT + 1) && GB0 >= 65, "second unit");
static_assert(KD >= 14 && KD <= 32, "late steps");

typedef u32 u32x16 __attribute__((ext_vector_type(16)));
typedef u32 u32x8 __attribute__((ext_vector_type(8)));
#define GAS __attribute__((address_space(1)))
template <class V> DI u64 kp(V v, int i) { return (u64)v[2 * i] | ((u64)v[2 * i + 1] << 32); }

template <bool ISA> DI void role(const int w, int tid, const u64 ka, const u32 j0, const u32 j1, const u64 tstart) {
  u32x16 k0;
  u32x8 k1;
  u32x4 k2;
  u32x2 k3;
  asm volatile("s_load_dwordx16 %0, %1, 0" : "=s"(k0) : "s"(ka) : "memory");
  if constexpr (ISA)
    asm volatile("s_load_dwordx8 %0, %3, 64\n\ts_load_dwordx4 %1, %3, 96\n\ts_load_dwordx2 %2, %3, 112" : "=&s"(k1), "=&s"(k2), "=&s"(k3) : "s"(ka) : "memory");
  if constexpr (ISA) asm volatile("; A" : "+v"(tid));
  else asm volatile("; B" : "+v"(tid));
  const int lane = tid & 63;
  const int b = blockIdx.x, xcd = b & 7, q8 = b >> 3;
  const int g = xcd & 3, r = (xcd >> 2) * 8 + (q8 >> 2), j = q8 & 3, s = 4 * g + j;
  const int u0 = 7 * g + ((0x5320 >> (4 * j)) & 15), u1 = 7 * g + ((0x6411 >> (4 * j)) & 15);
  const int rg = w & 3, h = lane >> 5, l31 = lane & 31;
  const int rl = lane >> 3, pos = lane & 7, gi = lane >> 4;
  const int blk = r * 4 + j;
  const int l16 = lane * 16;

  int ox = (r * 128 + s * 8 + w) * 1024 + l16;
  int ow = ((blk & 7) * 64 + w * 8 + rl) * 3584 + (7 * g + (blk < 56 ? blk >> 3 : 0)) * 128 + pos * 16;
  int ob = (s * 32 + r * 2 + (rg >> 1)) * 1024 + l16;
  const int ctc = ((rg >> 1) ? u1 : u0) * 64 + ((rg & 1) ^ (rg >> 1 & (j == 1))) * 32;
  int ol = (s * 32 + l31) * 2, od = (ctc + pos * 4 + gi) * 2;
  const int vxE = rl * 1024 + ((pos ^ (rl >> 1)) << 4), vxO = vxE ^ 64;
  const int vw1 = rl * 3584 + ((pos ^ ((rl & 2) << 1)) << 4);
  const bool xt = ISA || w < 6;
  int vmE = xt ? vxE : vw1, vmO = xt ? vxO : vw1;
  const int pstr = xt ? 8192 : 8 * 3584, cadv = xt ? 128 - 3 * 8192 : 40 * 3584;
  const int mb1 = ISA ? 0 : opqs(s * 32768), mb2 = ISA ? 0 : opqs((w - 6) * 32 * 3584 + u0 * 128);
  int mso = opqs((ISA ? (r * 128 + w * 32) * 1024 : (xt ? mb1 : mb2)) - cadv);
  const int lw4 = opqs(rg * 4096 + (ISA ? 0 : WB));
  const u32 msz = ISA ? 2048u * 1024u : opqs(xt ? 512u * 1024u : 512u * 3584u);
  u32x4 ax_, aw_, ab_ = {};
  u32 pa, pb, pc, pd;
  pin(ox, ow, vmE, vmO);
  if constexpr (ISA) pin(ob, ol, od);
  SB;
  if constexpr (ISA) asm volatile("s_waitcnt lgkmcnt(0)" : "+s"(k0), "+s"(k1), "+s"(k2), "+s"(k3) : "s"(j0), "s"(j1) : "memory");
  else asm volatile("s_waitcnt lgkmcnt(0)" : "+s"(k0) : "s"(j0), "s"(j1) : "memory");
  const u64 x = kp(k0, 0), w1 = kp(k0, 1), bre = kp(k0, 2), bim = kp(k0, 3), ws = kp(k0, 4), b1 = kp(k0, 5), lre = kp(k0, 6), lim = kp(k0, 7);
  const u64 ldt = kp(k1, 0), cre = kp(k1, 1), cim = kp(k1, 2), w2 = kp(k1, 3), dsk = kp(k2, 0), b2 = kp(k2, 1), out = kp(k3, 0);
  if constexpr (ISA) {
    asm volatile("global_load_ushort %0, %1, %2" : "=v"(pa) : "v"(ol), "s"(lre) : "memory");
    asm volatile("global_load_ushort %0, %1, %2" : "=v"(pb) : "v"(ol), "s"(lim) : "memory");
    asm volatile("global_load_ushort %0, %1, %2" : "=v"(pc) : "v"(ol), "s"(ldt) : "memory");
    asm volatile("global_load_ushort %0, %1, %2" : "=v"(pd) : "v"(od), "s"(b1) : "memory");
    asm volatile("global_load_dwordx4 %0, %1, %2" : "=v"(ab_) : "v"(ob), "s"((w & 1) ? bim : bre) : "memory");
  }
  asm volatile("global_load_dwordx4 %0, %1, %2" : "=v"(ax_) : "v"(ox), "s"(x) : "memory");
  asm volatile("global_load_dwordx4 %0, %1, %2" : "=v"(aw_) : "v"(ow), "s"(w1) : "memory");
  const i32x4 rm = mkrs(ISA ? x : (w == 4 ? bre : (w == 5 ? bim : w1)), msz);
#define PIECE(c, p) dma_s<RO[(c) % 3] + (p) * 1024>(rm, ((p) & 1) ? vmO : vmE, mso, (p) == 0 ? cadv : pstr, lw4)
  PIECE(0, 0); PIECE(0, 1); PIECE(0, 2); PIECE(0, 3);
  u64 tag;
  asm volatile("s_load_dwordx2 %0, %1, 0" : "=s"(tag) : "s"(ws) : "memory");
  const i32x4 rw = mkrs(ws, WS_END);
  const int sv = opq(lane == 0 ? MI_CNT : MI_JUNK + lane * 4);
  if constexpr (!ISA) LDS(u32, MI_CNT + l31 * 4) = 0u;
  const int ax = opq(l31 * 128 + (((h ^ (l31 >> 1)) & 7) << 4));
  const int tq = (lane >> 2) & 3, tp = lane & 3;
  int xa[4];
#pragma unroll
  for (int k = 0; k < 4; ++k) xa[k] = opq((ax ^ (k * 32)) + rg * 4096);
  const u32 ordl = opqs((j & 2) ? ((j & 1) ? 0x51640CBAu : 0x51A40876u) : ((j & 1) ? 0x71A60354u : 0x75A64210u));
  const u32 ordh = opqs((j & 2) ? ((j & 1) ? 0xD98327u : 0x9DC32Bu) : ((j & 1) ? 0xD9C82Bu : 0xD9C83Bu));
  auto done = [&]() AI {
  };
  u32 tlo, thi;
  auto stats = [&]() AI {
    asm volatile("s_waitcnt vmcnt(4) lgkmcnt(0)" : "+v"(ax_), "+v"(aw_), "+s"(tag)::"memory");
    if constexpr (ISA) pin(ab_);
    tag += 1ull;
    tlo = opqs((u32)tag);
    thi = opqs((u32)(tag >> 32));
    STAMP(1);
    PIECE(1, 0);
    {
      float sm = 0.f, sq = 0.f, bs = 0.f, y[8];
#pragma unroll
      for (int i = 0; i < 4; ++i) {
        sm = dot2(ax_[i], 0x3f803f80u, sm);
        sq = dot2(ax_[i], ax_[i], sq);
        if constexpr (ISA) bs = dot2(ab_[i], 0x3f803f80u, bs);
      }
      sm = add32(sm, sq);
      sm = add16(sm, ISA ? add32(bs, bs) : sm);
      pin(sm);
      SB;
      PIECE(1, 1);
#pragma unroll
      for (int i = 0; i < 4; ++i) {
        y[2 * i] = bf2f(aw_[i]);
        y[2 * i + 1] = bfh(aw_[i]);
      }
      LDS(float, MI_PS + w * 16 + gi * 4) = rsum(sm);
#pragma unroll
      for (int i = 0; i < 4; ++i) y[i] = add32(y[i], y[i + 4]);
      pin(y[0], y[1], y[2], y[3]);
      SB;
      PIECE(1, 2);
      const float z0 = dppadd<0x128>(add16(y[0], y[2])), z1 = dppadd<0x128>(add16(y[1], y[3]));
      LDS(f32x2, MI_PW + w * 256 + (pos * 8 + gi * 2) * 4) = f32x2{z0, z1};
      SB;
      PIECE(1, 3);
    }
    STAMP(2);
  };

  const i32x4 r1 = mkrs(w1, 512u * 3584u);
  const int w1b = w * 8 * 3584 + u1 * 128, lw1 = w * 1024;
  auto w1p = [&](auto Cc) AI {
    constexpr int c = decltype(Cc)::v;
    dma1<c * 64 * 3584, U1S[c] * 8192>(r1, vw1, w1b, lw1);
  };
  auto tile = [&](int p) AI { return (int)((opqs(p >= 8 ? ordh : ordl) >> (4 * (p & 7))) & 15); };
  const int trb = MI_TR + rg * 4608;
  const int svp = sv + rg * 16;
  const int ffb = (int)WS_FF + ((r * 4 + g) * 4 + rg) * 2560;

  if constexpr (ISA) {
    cf l1, l2, l3, l4, l32, l256, co2, lF, lL, mq[4];
    {
      asm volatile("s_waitcnt vmcnt(7)" : "+v"(pa), "+v"(pb), "+v"(pc), "+v"(pd)::"memory");
      const float lr = bf2f(pa), li = bf2f(pb), dt = rbf(expf(bf2f(pc))), er = expf(dt * lr), th = dt * li;
      const float n = __builtin_rintf(th * 0.636619772f);
      const float y = fmaf(n, 1.71512451e-15f, fmaf(n, 4.37113883e-8f, fmaf(n, -1.57079637f, th))), z = y * y;
      const float pc_ = fmaf(z * z, fmaf(z, fmaf(z, 2.44331571e-5f, -1.38873163e-3f), 4.16666457e-2f), fmaf(z, -0.5f, 1.0f));
      const float ps_ = fmaf(z * y, fmaf(z, fmaf(z, -1.95152959e-4f, 8.33216087e-3f), -1.66666546e-1f), y);
      const u32 qd = (u32)(int)n;
      float sn = asf(asu((qd & 1) ? pc_ : ps_) ^ ((qd & 2) << 30)), cs = asf(asu((qd & 1) ? ps_ : pc_) ^ (((qd + 1) & 2) << 30));
      if (__builtin_expect(__builtin_amdgcn_ballot_w64(!(fabsf(th) < 8192.f)) != 0, 0)) sincosf(th, &sn, &cs);
      l1 = cf{er * cs, er * sn};
      const float den = 2.0f / fmaf(lr, lr, li * li);
      co2 = pmul(cf{l1.x - 1.0f, l1.y}, cf{lr * den, -li * den});
      const cf one = {1.f, 0.f};
      l2 = csq(l1);
      l3 = pmul(l2, l1);
      l4 = csq(l2);
      const cf l8 = csq(l4);
      l32 = csq(csq(l8));
      const cf l64 = csq(l32), l128 = csq(l64);
      l256 = csq(l128);
      const cf l512 = csq(l256);
      lF = pmul(pmul(csel(rg & 1, l512, one), csel(rg & 2, csq(l512), one)), csel(h, l128, one));
      lL = pmul(csel(rg & 1, l32, one), csel(rg & 2, l64, one));
      mq[0] = pmul(co2, csel(h, l4, one));
      mq[1] = pmul(mq[0], l8);
      mq[2] = pmul(mq[1], l8);
      mq[3] = pmul(mq[2], l8);
      pin(l1, l2, l3, l4, l32, l256, co2, lF, lL, mq[0], mq[1], mq[2], mq[3]);
    }
    stats();
    f32x16 are = {}, aim = {};
    bf16x8 fxr[3], fbr[3], fbi[3];
    int xb[4];
#pragma unroll
    for (int k = 0; k < 4; ++k) xb[k] = opq((ax ^ (k * 32)) + WB);
    auto rdi = [&](int n) AI { fbi[n % 3] = ld128(xb[n & 3] + RO[(n >> 2) % 3] + 4096); };
    auto rdx = [&](int n) AI {
      fbr[n % 3] = ld128(xb[n & 3] + RO[(n >> 2) % 3]);
      fxr[n % 3] = ld128(xa[n & 3] + RO[(n >> 2) % 3]);
    };
    const i32x4 rcr = mkrs(cre, 512u * 1024u), rci = mkrs(cim, 512u * 1024u), r2 = mkrs(w2, 1792u * 1024u);
    const int lw2 = rg * 2048, w2b = (448 * g + rg * 8) * 1024 + j * 256;
    int vw2;
    auto w2t = [&](auto Pp) AI {
      constexpr int p = decltype(Pp)::v;
      const int sb = w2b + (tile(p) << 15);
      dma1<0, W2S[p] * 8192>(r2, vw2, sb, lw2);
      dma1<4096, W2S[p] * 8192 + 1024>(r2, vw2, sb, lw2);
    };
    const int cb0 = opqs((j * 128 + (((rg >> 1) ^ g) * 32) + (rg & 1) * 16) * 1024 + g * 256 + (j >> 1) * 128);
    auto ctile = [&](auto Pp) AI {
      constexpr int p = decltype(Pp)::v;
      const int sb = opqs(cb0) ^ ((p & 1) * 65536 + (p >> 2) * 128);
      dma1<0, (4 + p) * 8192>((p & 2) ? rci : rcr, vxE, sb, lw2);
      dma1<8192, (4 + p) * 8192 + 1024>((p & 2) ? rci : rcr, vxO, sb, lw2);
    };
    float t0, t1;
    int o1, o2, o3, a8, aw, at, lo0, lo1, fo;
    u32x2 gs;
    u32x4 gw0, gw1, gb;
    u32 nm;
    f32x4 rsv[4], nmv[4];
    const char* tch;
    const u64 csrc = (u64)((rg & 1) ? cim : cre);
    const int d0 = r - 1 - 4 * rg - h;
    const int gro = opqs((int)WS_GR + (s * 16 + r) * 512), grs = opqs((int)WS_GR + s * 8192);
    int cvo;
    auto gather = [&]() AI {
      asm volatile("buffer_load_dwordx2 %0, %4, %7, 0 offen sc1\n\tbuffer_load_dwordx4 %1, %5, %7, 0 offen sc1\n\t"
                   "buffer_load_dwordx4 %2, %5, %7, 0 offen offset:16 sc1\n\tbuffer_load_dwordx4 %3, %6, %7, 0 offen sc1"
                   : "=&v"(gs), "=&v"(gw0), "=&v"(gw1), "=&v"(gb)
                   : "v"(o1), "v"(o2), "v"(o3), "s"(rw)
                   : "memory");
    };
    const int xin = opqs((int)WS_X + (((r * 4 + j) * 4 + g) * 4 + rg) * 6144);
    u32x4 none;
    auto side = [&](auto Nn) AI {
      constexpr int n = decltype(Nn)::v;
      if constexpr (n == 3) {
        none = u32x4{~0u, ~0u, ~0u, ~0u};
        pin(none);
      }
      if constexpr (n >= 6 && n <= 16 && !(n & 1)) stg4<((n - 6) * 512) & 4095>(none, l16, rw, xin + (((n - 6) * 512) & 4096));
      if constexpr (n == 23) stg2<0>(u32x2{tlo, thi}, lane == 0 ? (int)WS_FI + (((r * 4 + j) * 4 + rg) * 4 + g) * 128 : OOB, rw);
      if constexpr (n == 1) {
        u32 val = 0;
        int o = OOB;
        if (w == 2) {
          const int a = opq(MI_PW + lane * 4);
          float c[8];
#pragma unroll
          for (int i = 0; i < 8; ++i) c[i] = LDS(float, a + i * 256);
          o = (blk < 56 ? (int)WS_GW + ((blk & 7) * 1792 + (7 * g + (blk >> 3)) * 64) * 8 : OOB) + lane * 8;
          val = asu(((c[0] + c[1]) + (c[2] + c[3])) + ((c[4] + c[5]) + (c[6] + c[7])));
        } else if (w == 3) {
          const f32x4 p = LDS(f32x4, MI_PS + pos * 16);
          const int oa = opqs((int)WS_GS + (r * 128 + s * 8) * 8), oc = opqs((int)WS_GB + (s * 32 + r * 2) * 16 - 64);
          o = (lane < 8 ? oa : (lane < 12 ? oc : OOB)) + lane * 8;
          const float m = p.x * (1.0f / 512.0f), v = fmaxf(fmaf(-m, m, p.z * (1.0f / 512.0f)), 0.f);
          val = lane < 8 ? pk2(m, __builtin_amdgcn_sqrtf(rbf(rbf(v) + 1e-5f))) : split2(p.y);
        }
        stg2<0>(u32x2{val, tlo}, o, rw);
      } else if constexpr (n == 4) {
        o1 = opq((int)WS_GS + (r * 128 + rg * 32 + l31) * 8);
        o2 = opq((int)WS_GW + (rl * 1792 + ctc + pos * 4) * 8);
        o3 = opq((int)WS_GB + (s * 32 + l31) * 16);
      } else if constexpr (n == 5) {
        vw2 = opq(gi * 1024 + (((lane & 15) ^ ((gi ^ g) << 2)) << 4));
        a8 = opq(MI_CT + l31 * 8);
        cvo = opq(lane < (w == 3 ? 32 : 0) ? l16 : OOB);
      } else if constexpr (n == 7) {
        aw = opq(MI_CT + rg * 256 + l31 * 8 + h * 4);
        at = opq(trb + l31 * 72 + h * 8);
      } else if constexpr (n == 8) {
        lo0 = opq(d0 >= 0 ? d0 * 512 + l31 * 16 : OOB);
        lo1 = opq(d0 >= 2 ? (d0 - 2) * 512 + l31 * 16 : OOB);
      } else if constexpr (n == 9) {
        const bool ln = lane < 14;
        const int oa = (g * 3584 + blk * 56 + rg * 14 + lane) * 128, oc = (blk * 8 + (rg >> 1) * 4 + (pos >> 1)) * 1024 + g * 256 + (pos & 1) * 128;
        const int o = ln ? oa : oc;
        const u64 p2 = (u64)w2, p3 = csrc;
        tch = (const char*)(ln ? p2 : p3) + o;
        pin(tch);
      } else if constexpr (n == 11) {
        const u32 nd = (0xCFFFFu & ~(0x11111u << j)) | (0xF00000u & ~(0x100000u << g));
        const int fi = opqs((int)WS_FI + ((r * 4 + j) * 4 + rg) * 512 - 2560), ff = opqs(ffb);
        fo = opq((((nd >> l31) & 1) > h) ? (lane < 20 ? ff : fi) + lane * 128 : OOB);
      } else if constexpr (n == 17) {
        gather();
      } else if constexpr (n == 21) {
        touch(tch, MI_JUNK);
      } else if constexpr (n == 22) {
        pin(gs, gw0, gw1, gb);
        _Pragma("nounroll") for (u32 it = 0; it < SPIN_CAP; ++it) {
          const u32 d = ((gs.y ^ tlo) | (gw0.y ^ tlo)) | ((gw0.w ^ tlo) | (gw1.y ^ tlo)) | ((gw1.w ^ tlo) | (gb.y ^ tlo)) | (gb.w ^ tlo);
          if (__builtin_expect(__builtin_amdgcn_ballot_w64(d != 0) == 0, 1)) break;
          gather();
          asm volatile("s_waitcnt vmcnt(0)" : "+v"(gs), "+v"(gw0), "+v"(gw1), "+v"(gb)::"memory");
        }
      } else if constexpr (n == 23) {
        t0 = dppadd<0x128>(add16(add32(asf(gw0.x), asf(gw1.x)), add32(asf(gw0.z), asf(gw1.z))));
        pin(t0);
      } else if constexpr (n == 24) {
        const u32 p = split2(t0);
        LDS(u32x2, MI_CT + (rg * 32 + pos * 4 + gi) * 8) = u32x2{pd | (p << 16), p >> 16};
      } else if constexpr (n == 25) {
        t0 = bfh(gs.x);
        t1 = __builtin_amdgcn_rcpf(t0);
        nm = (gs.x & 0xffffu) ^ 0x8000u;
        t1 = fmaf(fmaf(-t0, t1, 1.0f), t1, t1);
        pin(t1, nm);
      } else if constexpr (n == 26) {
        const float gc0 = -2.302208198f * t1;
        LDS(u32x4, MI_RC + (rg * 32 + l31) * 16) = u32x4{asu(gc0), asu(gc0 * 0.044715f * (t1 * t1)), (gs.x >> 16) | (nm << 16), nm};
      } else if constexpr (n == 27) {
        LDS(float, MI_RS + h * 512 + (rg * 32 + l31) * 4) = h ? bf2f(nm) * t1 : t1;
        nm = h ? 0u : nm | (nm << 16);
        pin(nm);
      } else if constexpr (n == 28) {
        are = MFMA32(BC(bf16x8, (u32x4{nm, 0u, 0u, 0u})), BC(bf16x8, (u32x4{gb.x, 0u, 0u, 0u})), are); pin(are);
        aim = MFMA32(BC(bf16x8, (u32x4{nm, 0u, 0u, 0u})), BC(bf16x8, (u32x4{gb.z, 0u, 0u, 0u})), aim); pin(aim);
      } else if constexpr (n == 29) {
        const int a = opq(MI_RS + (rg * 32 + h * 4) * 4);
#pragma unroll
        for (int q = 0; q < 4; ++q) {
          rsv[q] = LDS(f32x4, a + q * 32);
          nmv[q] = LDS(f32x4, a + 512 + q * 32);
        }
      }
    };
    SB;
    bar<4>();
    STAMP(3);
    rdi(0); rdx(0); rdi(1); rdx(1);
    sfor<0, 32>([&](auto Nn) AI {
      constexpr int n = decltype(Nn)::v, c = n >> 2, ks = n & 3;
      SB;
      are = MFMA32(fxr[n % 3], fbr[n % 3], are); pin(are);
      SB;
      if (ks != 2 && n + 2 < 32) rdi(n + 2);
      if constexpr (n < 2) PIECE(2, 2 * n);
      if constexpr (ks != 2) {
        if constexpr (n < 2) PIECE(2, 2 * n + 1);
        else if constexpr (n < 22) PIECE(((n - 2) >> 2) + 3, (n - 2) & 3);
        else if constexpr (n < 30) w1p(IC<n - 22>{});
        else w2t(IC<1>{});
      }
      SB;
      aim = MFMA32(fxr[n % 3], fbi[n % 3], aim); pin(aim);
      SB;
      if constexpr (ks == 2) {
        bar<abar(n)>();
        if (n + 2 < 32) rdi(n + 2);
      }
      if (n + 2 < 32) rdx(n + 2);
      if constexpr (ks == 2) {
        if constexpr (n < 22) PIECE(((n - 2) >> 2) + 3, 0);
        else if constexpr (n < 30) w1p(IC<n - 22>{});
        else w2t(IC<0>{});
      }
      SB;
      side(Nn);
    });
    SB;
    STAMP(4);
    PRIO(2);
    pin(rsv[0], rsv[1], rsv[2], rsv[3]);
    cf u[16], sq[4];
#pragma unroll
    for (int i = 0; i < 16; ++i) {
      float a = are[i] * rsv[i >> 2][i & 3], c = aim[i] * rsv[i >> 2][i & 3];
      pin(a, c);
      u[i] = cf{a, c};
    }
    {
#pragma unroll
      for (int k = 1; k < 4; ++k)
#pragma unroll
        for (int q = 0; q < 4; ++q) u[4 * q + k] = cfma(l1, u[4 * q + k - 1], u[4 * q + k]);
      cf S = {0.f, 0.f};
#pragma unroll
      for (int q = 0; q < 4; ++q) {
        float ar, ai, cr, ci;
        halves(u[4 * q + 3].x, ar, cr);
        halves(u[4 * q + 3].y, ai, ci);
        const cf Sa = S;
        S = q ? cfma(l4, S, cf{ar, ai}) : cf{ar, ai};
        sq[q] = csel(h, S, Sa);
        S = cfma(l4, S, cf{cr, ci});
      }
      LDS(f32x2, a8 + (MI_WT - MI_CT) + rg * 256) = S;
    }
    cf t4[4];
    auto meet = [&](auto Oo, int a) AI {
      constexpr int o = decltype(Oo)::v;
      sig<o>(sv);
      _Pragma("nounroll") for (u32 it = 0; it < (SPIN_CAP << 4); ++it) {
        const u32 c = peek<o>(sv);
#pragma unroll
        for (int i = 0; i < 4; ++i) t4[i] = *(volatile LAS f32x2*)(size_t)(a + i * 256);
        if ((u32)__builtin_amdgcn_readfirstlane(c) >= 4u) break;
      }
    };
    meet(IC<C_WT>{}, a8 + (MI_WT - MI_CT));
    cf lin;
    {
      const cf s1 = cfma(l32, t4[0], t4[1]), s2 = cfma(l32, s1, t4[2]), s3 = cfma(l32, s2, t4[3]);
      stg4<0>(u32x4{asu(s3.x), tlo, asu(s3.y), tlo}, cvo, rw, gro);
      PRIO(0);
      lin = csel(rg & 2, csel(rg & 1, s2, s1), csel(rg & 1, t4[0], cf{0.f, 0.f}));
    }
    STAMP(5);
    waitv<5>();
    sig<C_U1>(sv);
    ctile(IC<0>{}); ctile(IC<1>{});
    PRIO(2);
    pin(co2);
#pragma unroll
    for (int i = 0; i < 16; ++i) u[i] = cmul(co2, u[i]);
#pragma unroll
    for (int q = 0; q < 4; ++q) sq[q] = cmul(co2, sq[q]);
#pragma unroll
    for (int q = 0; q < 4; ++q) pin(u[4 * q], u[4 * q + 1], u[4 * q + 2], u[4 * q + 3], sq[q]);
    pin(lin);
    PRIO(0);
    ctile(IC<2>{}); ctile(IC<3>{});
    w2t(IC<2>{}); w2t(IC<3>{}); w2t(IC<4>{});
    u32x4 lb0, lb1;
    ldg4(lb0, lo0, rw, grs);
    ldg4(lb1, lo1, rw, grs);
    lwait<C_P1, 4>(sv);
    w2t(IC<5>{}); w2t(IC<6>{}); w2t(IC<7>{}); w2t(IC<8>{});
    STAMP(6);
    asm volatile("s_waitcnt vmcnt(8)" : "+v"(lb0), "+v"(lb1)::"memory");
    sig<C_LA>(sv);
    PRIO(2);
    _Pragma("nounroll") for (u32 it = 0; it < SPIN_CAP; ++it) {
      const bool bad = ((d0 >= 0) & (((lb0.y ^ tlo) | (lb0.w ^ tlo)) != 0)) | ((d0 >= 2) & (((lb1.y ^ tlo) | (lb1.w ^ tlo)) != 0));
      if (__builtin_amdgcn_ballot_w64(bad) == 0) break;
      ldg4(lb0, lo0, rw, grs);
      ldg4(lb1, lo1, rw, grs);
      asm volatile("s_waitcnt vmcnt(0)" : "+v"(lb0), "+v"(lb1)::"memory");
    }
    {
      cf a = cmul(lF, cfma(l256, cf{asf(lb1.x), asf(lb1.z)}, cf{asf(lb0.x), asf(lb0.z)}));
      settle(a);
      LDS(float, aw) = add32(a.x, a.y);
    }
    meet(IC<C_HP>{}, a8);
    STAMP(7);
    {
      float zr = (t4[0].x + t4[1].x) + (t4[2].x + t4[3].x);
      pin(zr);
      const cf z = cfma(lL, cf{zr, (t4[0].y + t4[1].y) + (t4[2].y + t4[3].y)}, lin);
#pragma unroll
      for (int q = 0; q < 4; ++q) {
        const cf t = cfma(mq[q], z, sq[q]);
        cf v[4];
#pragma unroll
        for (int k = 0; k < 4; ++k) v[k] = cfma(k == 0 ? l1 : (k == 1 ? l2 : (k == 2 ? l3 : l4)), t, u[4 * q + k]);
        LDS(u32x2, at + 16 * q) = u32x2{pk2(v[0].x, v[1].x), pk2(v[2].x, v[3].x)};
        LDS(u32x2, at + 2304 + 16 * q) = u32x2{pk2(-v[0].y, -v[1].y), pk2(-v[2].y, -v[3].y)};
      }
    }
    sig<C_PAIR>(svp);
    PRIO(0);
    STAMP(8);
    u32x2 pv;
    int prog = 0;
    auto seen = [&]() AI {
      pin(pv);
      const bool bad = (fo != OOB) & (((pv.x ^ tlo) | (pv.y ^ thi)) != 0);
      fo = bad ? fo : OOB;
      prog = __builtin_ctzll(__builtin_amdgcn_ballot_w64(bad) | (1ull << 24)) >> 2;
      *(volatile LAS u32*)(size_t)(svp + C_PAIR + 4) = (u32)prog;
    };
    auto watch = [&](auto Oo) AI {
      constexpr int o = decltype(Oo)::v;
      _Pragma("nounroll") for (u32 it = 0; it < SPIN_CAP; ++it) {
        if constexpr (o >= 0) {
          if ((u32)__builtin_amdgcn_readfirstlane(peek<o>(sv)) >= 4u) break;
        } else {
          if (prog == 6) break;
        }
        ldg2(pv, fo, rw);
        waitv<0>();
        seen();
      }
    };
    watch(IC<C_P2>{});
    ldg2(pv, fo, rw);
    ctile(IC<4>{}); ctile(IC<5>{}); ctile(IC<6>{}); ctile(IC<7>{});
    waitv<8>();
    sig<C_LB>(sv);
    seen();
    ldg2(pv, fo, rw);
    waitv<0>();
    sig<C_LC>(sv);
    seen();
    STAMP(9);
    watch(IC<C_P3>{});
    STAMP(10);
    ldg2(pv, fo, rw);
    w2t(IC<9>{}); w2t(IC<10>{}); w2t(IC<11>{}); w2t(IC<12>{}); w2t(IC<13>{});
    waitv<10>();
    seen();
    ldg2(pv, fo, rw);
    float ep[16];
    {
      const int col = j * 128 + g * 32 + l31;
      const GAS u16* xp = (const GAS u16*)x + (size_t)(r * 128 + rg * 32 + h * 4) * 512 + col;
      u32 xv[16], dv = ((const GAS u16*)dsk)[col], bv = ((const GAS u16*)b2)[col];
#pragma unroll
      for (int e = 0; e < 16; ++e) xv[e] = xp[((e & 3) + 8 * (e >> 2)) * 512];
      SB;
      waitv<18>();
      sig<C_LD>(sv);
      seen();
      SB;
#pragma unroll
      for (int e = 0; e < 16; ++e) {
        float xf = bf2f(xv[e]), xn = fmaf(xf, rsv[e >> 2][e & 3], nmv[e >> 2][e & 3]);
        pin(xn);
        xf += bf2f(bv);
        pin(xf);
        ep[e] = fmaf(xn, bf2f(dv), xf);
        pin(ep[e]);
      }
    }
    watch(IC<-1>{});
    STAMP(11);
    lwait<C_PAIR + 8, 1>(svp);
    STAMP(12);
    u32x4 xi[6];
    _Pragma("nounroll") for (u32 it = 0; it < SPIN_CAP; ++it) {
#pragma unroll
      for (int i = 0; i < 6; ++i) ldg4(xi[i], l16, rw, xin + i * 1024);
      asm volatile("s_waitcnt vmcnt(0)" : "+v"(xi[0]), "+v"(xi[1]), "+v"(xi[2]), "+v"(xi[3]), "+v"(xi[4]), "+v"(xi[5])::"memory");
      u32 m = 0;
#pragma unroll
      for (int i = 0; i < 6; ++i) m = max(max(max(max(m, xi[i].x), xi[i].y), xi[i].z), xi[i].w);
      if (__builtin_amdgcn_ballot_w64(m == ~0u) == 0) break;
    }
    STAMP(13);
#pragma unroll
    for (int i = 0; i < 6; ++i)
#pragma unroll
      for (int e = 0; e < 4; ++e) {
        ep[(i & 1) * 8 + 2 * e] += bf2f(xi[i][e]);
        ep[(i & 1) * 8 + 2 * e + 1] += bfh(xi[i][e]);
      }
#pragma unroll
    for (int e = 0; e < 16; ++e) pin(ep[e]);
    lwait<C_PAIR + 8, 2>(svp);
    STAMP(14);
    GAS float* op = (GAS float*)out + (size_t)(r * 128 + rg * 32 + h * 4) * 512 + j * 128 + g * 32 + l31;
#pragma unroll
    for (int q = 0; q < 4; ++q) {
      const f32x4 o = LDS(f32x4, trb + q * 1024 + l16);
#pragma unroll
      for (int k = 0; k < 4; ++k) op[(8 * q + k) * 512] = ep[4 * q + k] + o[k];
    }
    if (r == 15 && s == 0 && w == 0) stg2<0>(u32x2{tlo, thi}, lane == 0 ? 0 : OOB, rw);
    done();
  } else {
    stats();
    f32x16 a0[2] = {}, a1[2];
    bf16x8 xr[32], fw[3][2];
    int wa[2];
    {
      const int aw0 = (8 * h + tq) * 128 + ((tq >> 1) << 6) + (gi & 1) * 32 + tp * 8;
      wa[0] = opq(aw0 + WB);
      wa[1] = opq((aw0 ^ 64) + WB);
    }
    auto rdw = [&](int n, int t) AI {
      if (n == 32) wa[t] = opq((wa[t] + 16384) ^ (j == 1 ? 64 : 0));
      fw[n % 3][t] = ldtr(wa[t] + (n < 32 ? RO[(n >> 2) % 3] + 8192 : (U1S[(n >> 2) & 7] - 8) * 8192) + (n & 3) * 2048, 512);
    };
    auto rdx = [&](int n) AI { xr[n] = ld128(xa[n & 3] + RO[(n >> 2) % 3]); };
    u32x4 rc;
    float sd;
    u32x2 ct[4];
    u32x4 hf[44];
    float gz, gr, go;
    auto gelu = [&](const f32x16* a, int t, int o) AI {
      const int v = t >> 1;
      if (!(t & 1)) {
        if (v < 32) {
          const float x_ = a[v >> 4][v & 15];
          gz = __builtin_amdgcn_exp2f(fmaf(asf(rc.y), x_ * x_, asf(rc.x)) * x_);
          pin(gz);
        }
      } else {
        if (v < 32) gz = fmaf(gz, sd, sd);
        if (v > 0) {
          const float y = a[(v - 1) >> 4][(v - 1) & 15] * gr;
          if (v & 1) {
            go = y;
            pin(go);
          } else {
            u32 p = pk2(go, y);
            pin(p);
            const int i = (v - 2) >> 1, f = i >> 2;
            hf[o ? (f < 2 ? KT + f : 40 + f) : f][i & 3] = p;
          }
        }
        if (v < 32) {
          gr = __builtin_amdgcn_rcpf(gz);
          pin(gr);
        }
      }
    };
    const int hfo = (int)WS_HF + (r * 4 + rg) * 56 * 2048;
    const int hq = hfo + g * 14 * 2048, hso = (int)WS_HS + (r * 4 + rg) * 65536;
    const int fo = opq(lane == 0 ? ffb + j * 128 : OOB);
    SB;
    bar<4>();
    STAMP(3);
    rdw(0, 1); rdw(0, 0); rdx(0); rdw(1, 1); rdw(1, 0); rdx(1);
    sfor<0, 32>([&](auto Nn) AI {
      constexpr int n = decltype(Nn)::v, ks = n & 3;
      SB;
      a0[0] = MFMA32(fw[n % 3][0], xr[n], a0[0]); pin(a0[0]);
      SB;
      if (ks != 2) {
        rdw(n + 2, 1);
        rdw(n + 2, 0);
      }
      if constexpr (n < 2) PIECE(2, 2 * n);
      SB;
      a0[1] = MFMA32(fw[n % 3][1], xr[n], a0[1]); pin(a0[1]);
      SB;
      if (ks == 2) {
        bar<4>();
        STS(4, 5 + (n >> 2));
        if (n == 30) {
          rc = LDS(u32x4, MI_RC + (rg * 32 + l31) * 16);
          const int a = opq(MI_CT + l31 * 8);
#pragma unroll
          for (int t = 0; t < 4; ++t) ct[t] = LDS(u32x2, a + t * 256);
        }
        rdw(n + 2, 1);
        rdw(n + 2, 0);
      }
      if (n + 2 < 32) rdx(n + 2);
      if constexpr (n < 2) PIECE(2, 2 * n + 1);
      else if constexpr (n < 22) PIECE(((n - 2) >> 2) + 3, (n - 2) & 3);
      else if constexpr (n < 30) w1p(IC<n - 22>{});
    });
    SB;
    PRIO(1);
    {
      const bf16x8 cb = BC(bf16x8, (u32x4{h ? 0u : rc.z, h ? 0u : rc.w, 0u, 0u}));
      sd = bf2f(rc.z);
      a0[0] = MFMA32(BC(bf16x8, (u32x4{ct[0].x, ct[0].y, ct[1].x, ct[1].y})), cb, a0[0]); pin(a0[0]);
      a1[0] = MFMA32(BC(bf16x8, (u32x4{ct[2].x, ct[2].y, ct[3].x, ct[3].y})), cb, f32x16{}); pin(a1[0]);
      const bf16x8 cc = BC(bf16x8, (u32x4{0u, 0u, h ? 0u : rc.z, h ? 0u : rc.w}));
      a0[1] = MFMA32(BC(bf16x8, (u32x4{ct[0].x, ct[0].y, ct[1].x, ct[1].y})), cc, a0[1]); pin(a0[1]);
      a1[1] = MFMA32(BC(bf16x8, (u32x4{ct[2].x, ct[2].y, ct[3].x, ct[3].y})), cc, f32x16{}); pin(a1[1]);
    }
    STAMP(4);
    f32x16 acc[4] = {};
    bf16x8 wf[2][4];
    u32 pk = 0, pq = 0;
    int a2[2][4], ac[4], ta;
    auto rd2 = [&](int k, int i, int buf) AI {
      if ((k >= KS && k < KS + 4) || (k >= 22 && k < 34)) {
        const int part = k < 10 ? (k - KS) >> 1 : (k < 26 ? (k - 22) >> 1 : 2 + ((k - 26) >> 2));
        const int q = k < 10 ? k & 1 : (k < 26 ? 2 + (k & 1) : (k - 26) & 3);
        wf[buf][i] = ld128(ac[q] + part * 16384 + i * 4096);
      } else {
        const int p = k < 4 ? k >> 1 : (k < 10 ? 2 : (k < 22 ? (k - 4) >> 1 : (k - 16) >> 1));
        const int sl = W2S[p];
        wf[buf][i] = ldtr(a2[sl >= 12][i] + (sl >= 12 ? sl - 12 : sl) * 8192 + (k & 1) * 4096, 2048);
      }
    };
    auto event = [&](auto Ee) AI {
      constexpr int idx = decltype(Ee)::v, kind = EV.e[idx].kind, a = EV.e[idx].arg;
      if constexpr (kind == E_ST) {
        if constexpr (a < 4) stg4<a * 1024>(hf[a], l16, rw, hfo + u0 * 4096);
        else if constexpr (a < 6) stg4<(a - 4) * 1024>(hf[KT + a - 4], l16, rw, hfo + u1 * 4096 + (j == 1 ? 2048 : 0));
        else stg4<(a - 4) * 1024>(hf[36 + a], l16 | (j >= 2 ? 0 : OOB), rw, hfo + u1 * 4096);
      } else if constexpr (kind == E_SS) {
        stg4<a * 1024>(hf[KS + a], l16, rw, hso + s * 4096);
      } else if constexpr (kind == E_FL) {
        waitv<idx - flaglast(a) - 1>();
        stg2<a * 512>(u32x2{tlo, thi}, fo, rw);
      } else if constexpr (a >= 22 && a < 34) {
        constexpr int d = a < 26 ? 1 : 2 + (((a - 26) >> 1) & 1), part = a < 26 ? (a - 22) >> 1 : (a - 26) >> 2;
        ldg4o<part * 2048 + (a & 1) * 1024>(hf[a], l16, rw, hso + (s ^ d) * 4096);
      } else {
        ldg4o<(a & 1) * 1024>(hf[a], l16, rw, hq + tile(a < 22 ? (a - 4) >> 1 : (a - 16) >> 1) * 2048);
      }
    };
    auto hlast = [&]() AI {
      const int so = hq + 13 * 2048, jj = j;
      asm volatile("s_cmp_gt_u32 %[j], 1\n\ts_cbranch_scc1 1f\n\tbuffer_load_dwordx4 %0, %[v], %[r], %[s] offen sc1\n\t"
                   "buffer_load_dwordx4 %1, %[v], %[r], %[s] offen offset:1024 sc1\n1:"
                   : "+v"(hf[42]), "+v"(hf[43])
                   : [v] "v"(l16), [r] "s"(rw), [s] "s"(so), [j] "s"(jj)
                   : "memory", "scc");
    };
    auto fill = [&](auto Gg) AI {
      constexpr int G = decltype(Gg)::v;
#pragma unroll
      for (int t = gsub(G, GA0, GA1); t < gsub(G + 1, GA0, GA1); ++t) gelu(a0, t, 0);
#pragma unroll
      for (int t = gsub(G, GB0, GB1); t < gsub(G + 1, GB0, GB1); ++t) gelu(a1, t, 1);
      sfor<0, 5>([&](auto Cc) AI {
        constexpr int c = decltype(Cc)::v;
        if constexpr (G == chkgap(c)) chk<C_PAIR + 4, c + 1>(svp, pq);
        if constexpr (G == chkgap(c) - 1) pq = peek<C_PAIR + 4>(svp);
      });
      sfor<evfirst(G), evfirst(G + 1)>(event);
      if constexpr (G == chkgap(4) + 4) hlast();
    };
    sfor<32, 64>([&](auto Nn) AI {
      constexpr int n = decltype(Nn)::v, m = n - 32;
      SB;
      a1[0] = MFMA32(fw[n % 3][0], xr[m], a1[0]); pin(a1[0]);
      SB;
      if (m == 14) chk<C_U1, 8>(sv, pk);
      if (n + 2 < 64) rdw(n + 2, 1);
      if (m == 30) chk<C_LA, 4>(sv, pk);
      if (m >= 30) rd2(0, 2 * (m - 30), 0);
      fill(IC<2 * m>{});
      SB;
      a1[1] = MFMA32(fw[n % 3][1], xr[m], a1[1]); pin(a1[1]);
      SB;
      if (n + 2 < 64) rdw(n + 2, 0);
      if (m >= 30) rd2(0, 2 * (m - 30) + 1, 0);
      if (m == 3) {
        waitv<0>();
        sig<C_U1>(sv);
      }
      fill(IC<2 * m + 1>{});
      if (m == 13) pk = peek<C_U1>(sv);
      if (m == 15) sig<C_P1>(sv);
      if (m == 31) sig<C_P2>(sv);
      if (m == 29) pk = peek<C_LA>(sv);
      if (m == 28) {
        const int a = (4 * h + tq) * 256 + (tq << 6) + (gi & 1) * 32 + tp * 8;
#pragma unroll
        for (int i = 0; i < 4; ++i) a2[0][i] = opq(a ^ (i << 6));
      }
      if (m == 29) {
#pragma unroll
        for (int i = 0; i < 4; ++i) a2[1][i] = opq(a2[0][i] + 98304);
      }
      if (m == 30) {
#pragma unroll
        for (int i = 0; i < 4; ++i) ac[i] = opq(ax ^ (i * 32 + 32768) ^ ((j & 1) * 64));
      }
      if (m == 31) ta = opq(trb + (8 * h + tq) * 72 + (16 * (gi & 1) + 4 * tp) * 2);
      if (m == 7) STS(1, 5);
      if (m == 15) STS(1, 6);
      if (m == 23) STS(1, 7);
    });
    STS(0, 5); STS(1, 8);
    sfor<0, 44>([&](auto Kk) AI {
      constexpr int k = decltype(Kk)::v;
      if constexpr (k >= 10 && k < 42) asm volatile("s_waitcnt vmcnt(%1)" : "+v"(hf[k]) : "n"(evfirst(gap1(k)) - evfind(E_LD, k) - 1) : "memory");
      if constexpr (k == 42) asm volatile("s_waitcnt vmcnt(0)" : "+v"(hf[42]), "+v"(hf[43])::"memory");
      sfor<(k >= KD ? 1 : 0), 4>([&](auto Ii) AI {
        constexpr int i = decltype(Ii)::v;
        SB;
        acc[i] = MFMA32(BC(bf16x8, hf[k]), wf[k & 1][i], acc[i]); pin(acc[i]);
        SB;
        if (i == 1) {
          if (k == 12) chk<C_LB, 4>(sv, pk);
          if (k == 24) chk<C_LC, 4>(sv, pk);
          if (k == 32) chk<C_LD, 4>(sv, pk);
          if (k == 43) chk<C_PAIR + 4, 6>(svp, pk);
        }
        if (k + 1 < 44 && (i > 0 || k + 1 < KD)) rd2(k + 1, i, (k + 1) & 1);
        if constexpr (k == 43 && i > 1) {
          rd2(KD + i - 2, 0, 0);
          wf[0][i - 1] = wf[0][0];
        }
        if (k == KS - 1 && i == 1) {
          chk<C_PAIR, 1>(svp, pk);
#pragma unroll
          for (int f = 0; f < 4; ++f) hf[KS + f] = BC(u32x4, ldtr(ta + (f >> 1) * 2304 + (f & 1) * 16 * 72, 4 * 72));
        }
        fill(IC<gapof(k, i)>{});
        if (k == KS - 1 && i == 0) pk = peek<C_PAIR>(svp);
        if (i == 3) {
          if (k == 11) pk = peek<C_LB>(sv);
          if (k == 23) pk = peek<C_LC>(sv);
          if (k == 31) pk = peek<C_LD>(sv);
          if (k == 42) pk = peek<C_PAIR + 4>(svp);
          if (k == 13) sig<C_P3>(sv);
        }
      });
      if (k == 3) STS(1, 9);
      if (k == 5) STS(1, 10);
      if (k == 7) STS(1, 11);
      if (k == 9) STS(1, 12);
      if (k == 10) STS(1, 13);
      if (k == 12) STS(1, 14);
      if (k == 9) STS(0, 6);
      if (k == 15) STS(0, 7);
      if (k == 21) STS(0, 8);
      if (k == 27) STS(0, 9);
      if (k == 33) STS(0, 10);
      if (k == 39) STS(0, 11);
    });
    STS(0, 12);
    u32x4 xo;
    sfor<KD, 44>([&](auto Kk) AI {
      constexpr int k = decltype(Kk)::v, t = k - KD;
      SB;
      acc[0] = MFMA32(BC(bf16x8, hf[k]), wf[0][1 + t % 3], acc[0]); pin(acc[0]);
      SB;
      if (k + 2 < 44) {
        rd2(k + 2, 0, 0);
        wf[0][1 + (t + 2) % 3] = wf[0][0];
      }
      if constexpr (t < 12) {
        constexpr int i = 1 + (t >> 2), e = (t & 3) * 4;
        u32 p0 = min(pk2(acc[i][e], acc[i][e + 1]), 0xfffffffeu), p1 = min(pk2(acc[i][e + 2], acc[i][e + 3]), 0xfffffffeu);
        pin(p0, p1);
        xo[(t & 1) * 2] = p0;
        xo[(t & 1) * 2 + 1] = p1;
        if (t & 1) stg4<((t >> 1) & 1) * 1024>(xo, l16, rw, (int)WS_X + (((r * 4 + j) * 4 + (g ^ i)) * 4 + rg) * 6144 + (i - 1) * 2048);
      }
      if (t == 11) sig<C_PAIR + 8>(svp);
    });
    SB;
    STS(0, 13);
#pragma unroll
    for (int q = 0; q < 4; ++q) LDS(f32x4, trb + q * 1024 + l16) = f32x4{acc[0][4 * q], acc[0][4 * q + 1], acc[0][4 * q + 2], acc[0][4 * q + 3]};
    sig<C_PAIR + 8>(svp);
    done();
  }
}

__global__ __launch_bounds__(512, 2) void fused_ssm_ffn(const u16* __restrict__ x, const u16* __restrict__ w1, const u16* __restrict__ bre,
                                                         const u16* __restrict__ bim, char* __restrict__ ws, const u16* __restrict__ b1,
                                                         const u16* __restrict__ lre, const u16* __restrict__ lim, const u16* __restrict__ ldt,
                                                         const u16* __restrict__ cre, const u16* __restrict__ cim, const u16* __restrict__ w2,
                                                         const u16* __restrict__ dsk, const u16* __restrict__ b2, float* __restrict__ out) {
  u64 stp[1] = {0};
  STAMP(0);
  const u64 ka = (u64)__builtin_amdgcn_kernarg_segment_ptr();
  u32 j0, j1;
  asm volatile("s_load_dword %0, %2, 0\n\ts_load_dword %1, %2, 64" : "=&s"(j0), "=&s"(j1) : "s"(ka) : "memory");
  const int w = __builtin_amdgcn_readfirstlane(threadIdx.x >> 6);
  if (w < 4) role<true>(w, threadIdx.x, ka, j0, j1, stp[0]);
  else role<false>(w, threadIdx.x, ka, j0, j1, stp[0]);
}

extern "C" void kernel_launch(void* const* d_in, const int* in_sizes, int n_in, void* d_out, int out_size, void* d_ws, size_t ws_size,
                              hipStream_t stream) {
  (void)hipFuncSetAttribute((const void*)fused_ssm_ffn, hipFuncAttributeMaxDynamicSharedMemorySize, LDS_BYTES);
  fused_ssm_ffn<<<256, 512, LDS_BYTES, stream>>>((const u16*)d_in[0], (const u16*)d_in[10], (const u16*)d_in[3], (const u16*)d_in[4], (char*)d_ws,
                                                 (const u16*)d_in[11], (const u16*)d_in[1], (const u16*)d_in[2], (const u16*)d_in[7],
                                                 (const u16*)d_in[5], (const u16*)d_in[6], (const u16*)d_in[12], (const u16*)d_in[8],
                                                 (const u16*)d_in[13], (float*)d_out);
}
```

```cpp
#define HOLD 4000
#define HOLDAT 15
#include <hip/hip_runtime.h>
#include <hip/hip_bf16.h>

typedef unsigned short u16;
typedef unsigned u32;
typedef unsigned long long u64;
typedef short bf16x8 __attribute__((ext_vector_type(8)));
typedef short bf16x4 __attribute__((ext_vector_type(4)));
typedef float f32x16 __attribute__((ext_vector_type(16)));
typedef float f32x4 __attribute__((ext_vector_type(4)));
typedef float f32x2 __attribute__((ext_vector_type(2)));
typedef __bf16 bf16x2n __attribute__((ext_vector_type(2)));
typedef u32 u32x4 __attribute__((ext_vector_type(4)));
typedef u32 u32x2 __attribute__((ext_vector_type(2)));
typedef int i32x4 __attribute__((ext_vector_type(4)));
#define LAS __attribute__((address_space(3)))
#define DI __device__ __forceinline__
#define AI __attribute__((always_inline))
#define SB __builtin_amdgcn_sched_barrier(0)
#define PRIO(n) do { SB; asm volatile("s_setprio " #n); SB; } while (0)
#define MFMA32(a, b, c) __builtin_amdgcn_mfma_f32_32x32x16_bf16((a), (b), (c), 0, 0, 0)
#define BC(T, v) __builtin_bit_cast(T, v)
#define SC1 16

constexpr u32 WS_FF = 4096;
constexpr u32 WS_FI = WS_FF + 256 * 2560;
constexpr u32 WS_GR = WS_FI + 131072;
constexpr u32 WS_GS = WS_GR + 131072;
constexpr u32 WS_GB = WS_GS + 16384;
constexpr u32 WS_GW = WS_GB + 8192;
constexpr u32 WS_HF = 1310720;
constexpr u32 WS_HS = WS_HF + 16 * 4 * 56 * 2048;
constexpr u32 WS_X = WS_HS + 16 * 4 * 64 * 1024;
constexpr u32 WS_END = WS_X + 16 * 4 * 4 * 4 * 6144;
static_assert(WS_GW + 8 * 1792 * 8 <= WS_HF, "ws map");

constexpr int WB = 49152;
constexpr int RO[3] = {16384, 0, 32768};
constexpr int U1S[8] = {12, 13, 14, 15, 10, 11, 8, 9};
constexpr int W2S[14] = {16, 0, 1, 2, 3, 12, 13, 14, 15, 16, 0, 1, 2, 3};
constexpr int MI_TR = 139264, MI_PW = MI_TR, MI = MI_TR + 8 * 2304;
constexpr int MI_PS = MI, MI_CT = MI + 128, MI_RC = MI_CT + 1024, MI_RS = MI_RC + 2048, MI_NM = MI_RS + 512, MI_WT = MI_NM + 512, MI_HP = MI_WT + 1024,
              MI_CNT = MI_HP, MI_JUNK = MI_CNT + 128;
constexpr int MI_HQ = MI_CT;
constexpr int LDS_BYTES = 163840;
static_assert(MI_JUNK + 256 + 128 <= LDS_BYTES, "lds map");
constexpr int C_WT = 0, C_HP = 4, C_U1 = 8, C_LA = 12, C_LB = 16, C_LC = 20, C_LD = 24, C_P1 = 28, C_P2 = 32, C_P3 = 36, C_PAIR = 64;
constexpr int OOB = 0x40000000;
constexpr u32 SPIN_CAP = 1u << 18;
#define KD 22

#define STAMP(i)
#define SET 0
#define STS(set, i) do { if (SET == (set)) STAMP(i); } while (0)

DI float asf(u32 b) { return BC(float, b); }
DI u32 asu(float f) { return BC(u32, f); }
DI float bf2f(u32 b) { return asf(b << 16); }
DI float bfh(u32 b) { return asf(b & 0xffff0000u); }
DI u32 pk2(float a, float b) {
  f32x2 v = {a, b};
  return BC(u32, __builtin_convertvector(v, bf16x2n));
}
DI float rbf(float a) { return bf2f(pk2(a, a)); }
DI i32x4 mkrs(u64 a, u32 bytes) {
  i32x4 r = {(int)(u32)a, (int)((u32)(a >> 32) & 0xffffu), (int)bytes, 0x00020000};
  return r;
}
template <int SI, int LI> DI void dma1(i32x4 rs, int v, int sb, int lw) {
  int t;
  asm volatile("s_add_i32 m0, %[lw], %[li]\n\ts_add_i32 %[t], %[sb], %[si]\n\tbuffer_load_dwordx4 %[v], %[rs], %[t] offen lds"
               : [t] "=&s"(t)
               : [v] "v"(v), [rs] "s"(rs), [sb] "s"(sb), [lw] "s"(lw), [si] "n"(SI), [li] "n"(LI)
               : "memory", "scc");
}
template <int LI> DI void dma_s(i32x4 rs, int v, int& so, int ps, int lw) {
  asm volatile("s_add_i32 m0, %[lw], %[li]\n\ts_add_i32 %[t], %[t], %[ps]\n\tbuffer_load_dwordx4 %[v], %[rs], %[t] offen lds"
               : [t] "+s"(so)
               : [v] "v"(v), [rs] "s"(rs), [ps] "s"(ps), [lw] "s"(lw), [li] "n"(LI)
               : "memory", "scc");
}
DI void touch(const void* p, int lw) {
  asm volatile("s_mov_b32 m0, %1\n\ts_nop 0\n\tglobal_load_lds_dword %0, off" ::"v"(p), "s"(lw) : "memory");
}
template <int I> struct IC { static constexpr int v = I; };
template <int A, int B, class Fn> DI void sfor(Fn&& f) {
  if constexpr (A < B) {
    f(IC<A>{});
    sfor<A + 1, B>(f);
  }
}
typedef f32x2 cf;
template <class T> DI void pin1(T& v) { asm volatile("" : "+v"(v)); }
template <class... T> DI void pin(T&... v) { (pin1(v), ...); }
DI int opq(int v) { asm volatile("" : "+v"(v)); return v; }
DI int opqs(int v) { asm volatile("" : "+s"(v)); return v; }
template <int N> DI void bar() { asm volatile("s_waitcnt vmcnt(%0) lgkmcnt(0)\n\ts_barrier" ::"n"(N) : "memory"); }
template <int N> DI void waitv() { asm volatile("s_waitcnt vmcnt(%0)" ::"n"(N) : "memory"); }
DI void waitl() { asm volatile("s_waitcnt lgkmcnt(0)" ::: "memory"); }
DI bf16x8 ld128(int a) { return *(LAS bf16x8*)(size_t)a; }
DI bf16x8 ldtr(int a, int d) {
  bf16x4 lo = __builtin_amdgcn_ds_read_tr16_b64_v4i16((LAS bf16x4*)(size_t)a);
  bf16x4 hi = __builtin_amdgcn_ds_read_tr16_b64_v4i16((LAS bf16x4*)(size_t)(a + d));
  return __builtin_shufflevector(lo, hi, 0, 1, 2, 3, 4, 5, 6, 7);
}
#define LDS(T, a) (*(LAS T*)(size_t)(a))
DI float add32(float a, float b) {
  auto r = __builtin_amdgcn_permlane32_swap(asu(a), asu(b), false, false);
  return asf(r[0]) + asf(r[1]);
}
DI float add16(float a, float b) {
  auto r = __builtin_amdgcn_permlane16_swap(asu(a), asu(b), false, false);
  return asf(r[0]) + asf(r[1]);
}
template <int C> DI float dppadd(float v) { return v + asf((u32)__builtin_amdgcn_update_dpp(0, (int)asu(v), C, 0xf, 0xf, true)); }
DI float rsum(float v) {
  v = dppadd<0xB1>(v);
  v = dppadd<0x4E>(v);
  v = dppadd<0x141>(v);
  return dppadd<0x140>(v);
}
DI void halves(float v, float& a, float& b) {
  auto r = __builtin_amdgcn_permlane32_swap(asu(v), asu(v), false, false);
  a = asf(r[0]);
  b = asf(r[1]);
}
DI u32 split2(float c) {
  return pk2(c, c - rbf(c));
}
DI float dot2(u32 a, u32 b, float c) { return __builtin_amdgcn_fdot2_f32_bf16(BC(bf16x2n, a), BC(bf16x2n, b), c, false); }
DI cf pfma(cf a, cf b, cf c) {
  asm("v_pk_fma_f32 %0, %1, %2, %0 op_sel_hi:[0,1,1]\n\tv_pk_fma_f32 %0, %1, %2, %0 op_sel:[1,1,0] op_sel_hi:[1,0,1] neg_lo:[1,0,0]"
      : "+&v"(c)
      : "v"(a), "v"(b));
  return c;
}
DI cf pmul(cf a, cf b) {
  cf t;
  asm("v_pk_mul_f32 %0, %1, %2 op_sel_hi:[0,1]\n\tv_pk_fma_f32 %0, %1, %2, %0 op_sel:[1,1,0] op_sel_hi:[1,0,1] neg_lo:[1,0,0]"
      : "=&v"(t)
      : "v"(a), "v"(b));
  return t;
}
DI cf csq(cf a) { return pmul(a, a); }
DI float sfma(float a, float b, float c) { asm("v_fma_f32 %0, %1, %2, %3" : "=v"(c) : "v"(a), "v"(b), "v"(c)); return c; }
DI cf cfma(cf a, cf b, cf c) {
  float x = c.x, y = c.y;
  asm("v_fmac_f32 %0, %2, %4\n\tv_fmac_f32 %1, %2, %5\n\tv_fma_f32 %0, -%3, %5, %0\n\tv_fmac_f32 %1, %3, %4"
      : "+&v"(x), "+&v"(y)
      : "v"(a.x), "v"(a.y), "v"(b.x), "v"(b.y));
  return cf{x, y};
}
DI cf cmul(cf a, cf b) {
  float x, y;
  asm("v_mul_f32 %0, %2, %4\n\tv_mul_f32 %1, %2, %5\n\tv_fma_f32 %0, -%3, %5, %0\n\tv_fmac_f32 %1, %3, %4"
      : "=&v"(x), "=&v"(y)
      : "v"(a.x), "v"(a.y), "v"(b.x), "v"(b.y));
  return cf{x, y};
}
DI cf csel(bool c, cf a, cf b) { return cf{c ? a.x : b.x, c ? a.y : b.y}; }
DI void settle(cf& a) { asm volatile("s_nop 1" : "+v"(a)); }
template <int O> DI void sig(int sv) { asm volatile("ds_inc_u32 %0, %0 offset:%1" ::"v"(sv), "n"(O) : "memory"); }
template <int O> DI u32 peek(int sv) { return *(volatile LAS u32*)(size_t)(sv + O); }
template <int O, int N> DI void chk(int sv, u32 v) {
  int t, c;
  asm volatile("v_readfirstlane_b32 %[t], %[v]\n\ts_cmp_ge_u32 %[t], %[n]\n\ts_cbranch_scc1 2f\n\ts_mov_b32 %[c], 0x400000\n"
               "1:\n\ts_sleep 1\n\tds_read_b32 %[v], %[a] offset:%[o]\n\ts_sub_u32 %[c], %[c], 1\n\ts_waitcnt lgkmcnt(0)\n\ts_cbranch_scc1 2f\n\t"
               "v_readfirstlane_b32 %[t], %[v]\n\ts_cmp_lt_u32 %[t], %[n]\n\ts_cbranch_scc1 1b\n2:"
               : [t] "=&s"(t), [c] "=&s"(c), [v] "+v"(v)
               : [a] "v"(sv), [o] "n"(O), [n] "n"(N)
               : "memory", "scc");
}
template <int O, int N> DI void lwait(int sv) { chk<O, N>(sv, peek<O>(sv)); }
DI void ldg4(u32x4& d, int vo, i32x4 rs, int so) {
  asm volatile("buffer_load_dwordx4 %0, %1, %2, %3 offen sc1" : "=v"(d) : "v"(vo), "s"(rs), "s"(so) : "memory");
}
template <int O> DI void ldg4o(u32x4& d, int vo, i32x4 rs, int so) {
  asm volatile("buffer_load_dwordx4 %0, %1, %2, %3 offen offset:%4 sc1" : "=v"(d) : "v"(vo), "s"(rs), "s"(so), "n"(O) : "memory");
}
DI void ldg2(u32x2& d, int vo, i32x4 rs) { asm volatile("buffer_load_dwordx2 %0, %1, %2, 0 offen sc1" : "=v"(d) : "v"(vo), "s"(rs) : "memory"); }
template <int O> DI void stg4(u32x4 d, int vo, i32x4 rs, int so) {
  asm volatile("buffer_store_dwordx4 %0, %1, %2, %3 offen offset:%4 sc1" ::"v"(d), "v"(vo), "s"(rs), "s"(so), "n"(O) : "memory");
}
template <int O> DI void stg2(u32x2 d, int vo, i32x4 rs) {
  asm volatile("buffer_store_dwordx2 %0, %1, %2, 0 offen offset:%3 sc1" ::"v"(d), "v"(vo), "s"(rs), "n"(O) : "memory");
}

constexpr int aextra(int n) { return n == 17 ? 4 : ((n == 1 || n == 21 || n == 23 || (n >= 6 && n <= 16 && !(n & 1))) ? 1 : 0); }
constexpr int abar(int nb) {
  int c = (nb >= 6 && nb != 22) ? aextra(nb - 5) : 0;
  for (int m = nb < 6 ? 0 : nb - 4; m < nb; ++m) c += (m < 2 ? 2 : 1) + aextra(m);
  return c;
}
#define KS 6
#define KT 4
#define GA0 0
#define GA1 56
#define GB0 66
#define GB1 101
#define DF 16
#define LD0 24
#define LD1 24
#define LD2 26
#define LD3 26
#define LD4 18
constexpr int gapof(int k, int i) { return 64 + (k < KD ? 4 * k + i : 4 * KD + 3 * (k - KD) + i - 1); }
constexpr int gap1(int k) { return gapof(k, k < KD ? 0 : 1); }
constexpr int gsub(int g, int g0, int g1) { return g <= g0 ? 0 : (g >= g1 ? 66 : ((g - g0) * 66 + (g1 - g0) - 1) / (g1 - g0)); }
constexpr int gdone(int t, int g0, int g1) { int g = g0; while (gsub(g + 1, g0, g1) <= t) ++g; return g; }
enum { E_ST, E_SS, E_FL, E_LD };
struct Ev { int gap, kind, arg; };
struct EvTab { Ev e[64]; int n; };
constexpr int GK[6] = {10, 16, 22, 34, 40, 42};
constexpr int LDA[5] = {LD0, LD1, LD2, LD3, LD4};
constexpr int chkgap(int g) { return gap1(GK[g]) - LDA[g]; }
constexpr EvTab mkev() {
  EvTab t{};
  int sg[8] = {};
  for (int f = 0; f < 8; ++f) {
    sg[f] = f < 4 ? gdone(16 * f + 17, GA0, GA1) : gdone(16 * (f - 4) + 17, GB0, GB1);
    t.e[t.n++] = Ev{sg[f], E_ST, f};
  }
  for (int f = 0; f < 4; ++f) t.e[t.n++] = Ev{gap1(KS) + 2 * f, E_SS, f};
  t.e[t.n++] = Ev{sg[1] + DF, E_FL, 0};
  t.e[t.n++] = Ev{sg[3] + DF, E_FL, 1};
  t.e[t.n++] = Ev{gap1(KS) + 6 + DF, E_FL, 2};
  t.e[t.n++] = Ev{sg[5] + DF, E_FL, 3};
  t.e[t.n++] = Ev{sg[7] + DF, E_FL, 4};
  for (int g = 0; g < 5; ++g)
    for (int k = GK[g]; k < GK[g + 1]; ++k) t.e[t.n++] = Ev{chkgap(g) + 2 * (k - GK[g]), E_LD, k};
  for (int a = 1; a < t.n; ++a)
    for (int c = a; c > 0 && t.e[c - 1].gap > t.e[c].gap; --c) {
      const Ev x = t.e[c];
      t.e[c] = t.e[c - 1];
      t.e[c - 1] = x;
    }
  return t;
}
constexpr EvTab EV = mkev();
constexpr int evfirst(int g) { int a = 0; while (a < EV.n && EV.e[a].gap < g) ++a; return a; }
constexpr int evfind(int kind, int arg) { int a = 0; while (EV.e[a].kind != kind || EV.e[a].arg != arg) ++a; return a; }
constexpr int flaglast(int g) { return g == 2 ? evfind(E_SS, 3) : evfind(E_ST, g < 2 ? 2 * g + 1 : 2 * g - 1); }
static_assert(gdone(33, GA0, GA1) < gap1(1) && gdone(65, GA0, GA1) < gap1(3) && GA1 <= GB0, "first unit");
static_assert(gdone(17, GB0, GB1) < gap1(KT) && gdone(33, GB0, GB1) < gap1(KT + 1) && GB0 >= 65, "second unit");
static_assert(KD >= 14 && KD <= 32, "late steps");

typedef u32 u32x16 __attribute__((ext_vector_type(16)));
typedef u32 u32x8 __attribute__((ext_vector_type(8)));
#define GAS __attribute__((address_space(1)))
template <class V> DI u64 kp(V v, int i) { return (u64)v[2 * i] | ((u64)v[2 * i + 1] << 32); }

template <bool ISA> DI void role(const int w, int tid, const u64 ka, const u32 j0, const u32 j1, const u64 tstart) {
  u32x16 k0;
  u32x8 k1;
  u32x4 k2;
  u32x2 k3;
  asm volatile("s_load_dwordx16 %0, %1, 0" : "=s"(k0) : "s"(ka) : "memory");
  if constexpr (ISA)
    asm volatile("s_load_dwordx8 %0, %3, 64\n\ts_load_dwordx4 %1, %3, 96\n\ts_load_dwordx2 %2, %3, 112" : "=&s"(k1), "=&s"(k2), "=&s"(k3) : "s"(ka) : "memory");
  if constexpr (ISA) asm volatile("; A" : "+v"(tid));
  else asm volatile("; B" : "+v"(tid));
  const int lane = tid & 63;
  const int b = blockIdx.x, xcd = b & 7, q8 = b >> 3;
  const int g = xcd & 3, r = (xcd >> 2) * 8 + (q8 >> 2), j = q8 & 3, s = 4 * g + j;
  const int u0 = 7 * g + ((0x5320 >> (4 * j)) & 15), u1 = 7 * g + ((0x6411 >> (4 * j)) & 15);
  const int rg = w & 3, h = lane >> 5, l31 = lane & 31;
  const int rl = lane >> 3, pos = lane & 7, gi = lane >> 4;
  const int blk = r * 4 + j;
  const int l16 = lane * 16;

  int ox = (r * 128 + s * 8 + w) * 1024 + l16;
  int ow = ((blk & 7) * 64 + w * 8 + rl) * 3584 + (7 * g + (blk < 56 ? blk >> 3 : 0)) * 128 + pos * 16;
  int ob = (s * 32 + r * 2 + (rg >> 1)) * 1024 + l16;
  const int ctc = ((rg >> 1) ? u1 : u0) * 64 + ((rg & 1) ^ (rg >> 1 & (j == 1))) * 32;
  int ol = (s * 32 + l31) * 2, od = (ctc + pos * 4 + gi) * 2;
  const int vxE = rl * 1024 + ((pos ^ (rl >> 1)) << 4), vxO = vxE ^ 64;
  const int vw1 = rl * 3584 + ((pos ^ ((rl & 2) << 1)) << 4);
  const bool xt = ISA || w < 6;
  int vmE = xt ? vxE : vw1, vmO = xt ? vxO : vw1;
  const int pstr = xt ? 8192 : 8 * 3584, cadv = xt ? 128 - 3 * 8192 : 40 * 3584;
  const int mb1 = ISA ? 0 : opqs(s * 32768), mb2 = ISA ? 0 : opqs((w - 6) * 32 * 3584 + u0 * 128);
  int mso = opqs((ISA ? (r * 128 + w * 32) * 1024 : (xt ? mb1 : mb2)) - cadv);
  const int lw4 = opqs(rg * 4096 + (ISA ? 0 : WB));
  const u32 msz = ISA ? 2048u * 1024u : opqs(xt ? 512u * 1024u : 512u * 3584u);
  u32x4 ax_, aw_, ab_ = {};
  u32 pa, pb, pc, pd;
  pin(ox, ow, vmE, vmO);
  if constexpr (ISA) pin(ob, ol, od);
  SB;
  if constexpr (ISA) asm volatile("s_waitcnt lgkmcnt(0)" : "+s"(k0), "+s"(k1), "+s"(k2), "+s"(k3) : "s"(j0), "s"(j1) : "memory");
  else asm volatile("s_waitcnt lgkmcnt(0)" : "+s"(k0) : "s"(j0), "s"(j1) : "memory");
  const u64 x = kp(k0, 0), w1 = kp(k0, 1), bre = kp(k0, 2), bim = kp(k0, 3), ws = kp(k0, 4), b1 = kp(k0, 5), lre = kp(k0, 6), lim = kp(k0, 7);
  const u64 ldt = kp(k1, 0), cre = kp(k1, 1), cim = kp(k1, 2), w2 = kp(k1, 3), dsk = kp(k2, 0), b2 = kp(k2, 1), out = kp(k3, 0);
  if constexpr (ISA) {
    asm volatile("global_load_ushort %0, %1, %2" : "=v"(pa) : "v"(ol), "s"(lre) : "memory");
    asm volatile("global_load_ushort %0, %1, %2" : "=v"(pb) : "v"(ol), "s"(lim) : "memory");
    asm volatile("global_load_ushort %0, %1, %2" : "=v"(pc) : "v"(ol), "s"(ldt) : "memory");
    asm volatile("global_load_ushort %0, %1, %2" : "=v"(pd) : "v"(od), "s"(b1) : "memory");
    asm volatile("global_load_dwordx4 %0, %1, %2" : "=v"(ab_) : "v"(ob), "s"((w & 1) ? bim : bre) : "memory");
  }
  asm volatile("global_load_dwordx4 %0, %1, %2" : "=v"(ax_) : "v"(ox), "s"(x) : "memory");
  asm volatile("global_load_dwordx4 %0, %1, %2" : "=v"(aw_) : "v"(ow), "s"(w1) : "memory");
  const i32x4 rm = mkrs(ISA ? x : (w == 4 ? bre : (w == 5 ? bim : w1)), msz);
#define PIECE(c, p) dma_s<RO[(c) % 3] + (p) * 1024>(rm, ((p) & 1) ? vmO : vmE, mso, (p) == 0 ? cadv : pstr, lw4)
  PIECE(0, 0); PIECE(0, 1); PIECE(0, 2); PIECE(0, 3);
  u64 tag;
  asm volatile("s_load_dwordx2 %0, %1, 0" : "=s"(tag) : "s"(ws) : "memory");
  const i32x4 rw = mkrs(ws, WS_END);
  const int sv = opq(lane == 0 ? MI_CNT : MI_JUNK + lane * 4);
  if constexpr (!ISA) LDS(u32, MI_CNT + l31 * 4) = 0u;
  const int ax = opq(l31 * 128 + (((h ^ (l31 >> 1)) & 7) << 4));
  const int tq = (lane >> 2) & 3, tp = lane & 3;
  int xa[4];
#pragma unroll
  for (int k = 0; k < 4; ++k) xa[k] = opq((ax ^ (k * 32)) + rg * 4096);
  const u32 ordl = opqs((j & 2) ? ((j & 1) ? 0x51640CBAu : 0x51A40876u) : ((j & 1) ? 0x71A60354u : 0x75A64210u));
  const u32 ordh = opqs((j & 2) ? ((j & 1) ? 0xD98327u : 0x9DC32Bu) : ((j & 1) ? 0xD9C82Bu : 0xD9C83Bu));
  auto hold = [&](int at) AI {
    if (at == HOLDAT) {
      u64 t;
      do { asm volatile("s_sleep 2\n\ts_memrealtime %0\n\ts_waitcnt lgkmcnt(0)" : "=s"(t)::"memory"); } while (t < tstart + HOLD);
    }
  };
  auto done = [&]() AI {
    hold(15);
  };
  u32 tlo, thi;
  auto stats = [&]() AI {
    asm volatile("s_waitcnt vmcnt(4) lgkmcnt(0)" : "+v"(ax_), "+v"(aw_), "+s"(tag)::"memory");
    if constexpr (ISA) pin(ab_);
    tag += 1ull;
    tlo = opqs((u32)tag);
    thi = opqs((u32)(tag >> 32));
    STAMP(1);
    PIECE(1, 0);
    {
      float sm = 0.f, sq = 0.f, bs = 0.f, y[8];
#pragma unroll
      for (int i = 0; i < 4; ++i) {
        sm = dot2(ax_[i], 0x3f803f80u, sm);
        sq = dot2(ax_[i], ax_[i], sq);
        if constexpr (ISA) bs = dot2(ab_[i], 0x3f803f80u, bs);
      }
      sm = add32(sm, sq);
      sm = add16(sm, ISA ? add32(bs, bs) : sm);
      pin(sm);
      SB;
      PIECE(1, 1);
#pragma unroll
      for (int i = 0; i < 4; ++i) {
        y[2 * i] = bf2f(aw_[i]);
        y[2 * i + 1] = bfh(aw_[i]);
      }
      LDS(float, MI_PS + w * 16 + gi * 4) = rsum(sm);
#pragma unroll
      for (int i = 0; i < 4; ++i) y[i] = add32(y[i], y[i + 4]);
      pin(y[0], y[1], y[2], y[3]);
      SB;
      PIECE(1, 2);
      const float z0 = dppadd<0x128>(add16(y[0], y[2])), z1 = dppadd<0x128>(add16(y[1], y[3]));
      LDS(f32x2, MI_PW + w * 256 + (pos * 8 + gi * 2) * 4) = f32x2{z0, z1};
      SB;
      PIECE(1, 3);
    }
    STAMP(2);
  };

  const i32x4 r1 = mkrs(w1, 512u * 3584u);
  const int w1b = w * 8 * 3584 + u1 * 128, lw1 = w * 1024;
  auto w1p = [&](auto Cc) AI {
    constexpr int c = decltype(Cc)::v;
    dma1<c * 64 * 3584, U1S[c] * 8192>(r1, vw1, w1b, lw1);
  };
  auto tile = [&](int p) AI { return (int)((opqs(p >= 8 ? ordh : ordl) >> (4 * (p & 7))) & 15); };
  const int trb = MI_TR + rg * 4608;
  const int svp = sv + rg * 16;
  const int ffb = (int)WS_FF + ((r * 4 + g) * 4 + rg) * 2560;

  if constexpr (ISA) {
    cf l1, l2, l3, l4, l32, l256, co2, lF, lL, mq[4];
    {
      asm volatile("s_waitcnt vmcnt(7)" : "+v"(pa), "+v"(pb), "+v"(pc), "+v"(pd)::"memory");
      const float lr = bf2f(pa), li = bf2f(pb), dt = rbf(expf(bf2f(pc))), er = expf(dt * lr), th = dt * li;
      const float n = __builtin_rintf(th * 0.636619772f);
      const float y = fmaf(n, 1.71512451e-15f, fmaf(n, 4.37113883e-8f, fmaf(n, -1.57079637f, th))), z = y * y;
      const float pc_ = fmaf(z * z, fmaf(z, fmaf(z, 2.44331571e-5f, -1.38873163e-3f), 4.16666457e-2f), fmaf(z, -0.5f, 1.0f));
      const float ps_ = fmaf(z * y, fmaf(z, fmaf(z, -1.95152959e-4f, 8.33216087e-3f), -1.66666546e-1f), y);
      const u32 qd = (u32)(int)n;
      float sn = asf(asu((qd & 1) ? pc_ : ps_) ^ ((qd & 2) << 30)), cs = asf(asu((qd & 1) ? ps_ : pc_) ^ (((qd + 1) & 2) << 30));
      if (__builtin_expect(__builtin_amdgcn_ballot_w64(!(fabsf(th) < 8192.f)) != 0, 0)) sincosf(th, &sn, &cs);
      l1 = cf{er * cs, er * sn};
      const float den = 2.0f / fmaf(lr, lr, li * li);
      co2 = pmul(cf{l1.x - 1.0f, l1.y}, cf{lr * den, -li * den});
      const cf one = {1.f, 0.f};
      l2 = csq(l1);
      l3 = pmul(l2, l1);
      l4 = csq(l2);
      const cf l8 = csq(l4);
      l32 = csq(csq(l8));
      const cf l64 = csq(l32), l128 = csq(l64);
      l256 = csq(l128);
      const cf l512 = csq(l256);
      lF = pmul(pmul(csel(rg & 1, l512, one), csel(rg & 2, csq(l512), one)), csel(h, l128, one));
      lL = pmul(csel(rg & 1, l32, one), csel(rg & 2, l64, one));
      mq[0] = pmul(co2, csel(h, l4, one));
      mq[1] = pmul(mq[0], l8);
      mq[2] = pmul(mq[1], l8);
      mq[3] = pmul(mq[2], l8);
      pin(l1, l2, l3, l4, l32, l256, co2, lF, lL, mq[0], mq[1], mq[2], mq[3]);
    }
    stats();
    f32x16 are = {}, aim = {};
    bf16x8 fxr[3], fbr[3], fbi[3];
    int xb[4];
#pragma unroll
    for (int k = 0; k < 4; ++k) xb[k] = opq((ax ^ (k * 32)) + WB);
    auto rdi = [&](int n) AI { fbi[n % 3] = ld128(xb[n & 3] + RO[(n >> 2) % 3] + 4096); };
    auto rdx = [&](int n) AI {
      fbr[n % 3] = ld128(xb[n & 3] + RO[(n >> 2) % 3]);
      fxr[n % 3] = ld128(xa[n & 3] + RO[(n >> 2) % 3]);
    };
    const i32x4 rcr = mkrs(cre, 512u * 1024u), rci = mkrs(cim, 512u * 1024u), r2 = mkrs(w2, 1792u * 1024u);
    const int lw2 = rg * 2048, w2b = (448 * g + rg * 8) * 1024 + j * 256;
    int vw2;
    auto w2t = [&](auto Pp) AI {
      constexpr int p = decltype(Pp)::v;
      const int sb = w2b + (tile(p) << 15);
      dma1<0, W2S[p] * 8192>(r2, vw2, sb, lw2);
      dma1<4096, W2S[p] * 8192 + 1024>(r2, vw2, sb, lw2);
    };
    const int cb0 = opqs((j * 128 + (((rg >> 1) ^ g) * 32) + (rg & 1) * 16) * 1024 + g * 256 + (j >> 1) * 128);
    auto ctile = [&](auto Pp) AI {
      constexpr int p = decltype(Pp)::v;
      const int sb = opqs(cb0) ^ ((p & 1) * 65536 + (p >> 2) * 128);
      dma1<0, (4 + p) * 8192>((p & 2) ? rci : rcr, vxE, sb, lw2);
      dma1<8192, (4 + p) * 8192 + 1024>((p & 2) ? rci : rcr, vxO, sb, lw2);
    };
    float t0, t1;
    int o1, o2, o3, a8, aw, at, lo0, lo1, fo;
    u32x2 gs;
    u32x4 gw0, gw1, gb;
    u32 nm;
    f32x4 rsv[4], nmv[4];
    const char* tch;
    const u64 csrc = (u64)((rg & 1) ? cim : cre);
    const int d0 = r - 1 - 4 * rg - h;
    const int gro = opqs((int)WS_GR + (s * 16 + r) * 512), grs = opqs((int)WS_GR + s * 8192);
    int cvo;
    auto gather = [&]() AI {
      asm volatile("buffer_load_dwordx2 %0, %4, %7, 0 offen sc1\n\tbuffer_load_dwordx4 %1, %5, %7, 0 offen sc1\n\t"
                   "buffer_load_dwordx4 %2, %5, %7, 0 offen offset:16 sc1\n\tbuffer_load_dwordx4 %3, %6, %7, 0 offen sc1"
                   : "=&v"(gs), "=&v"(gw0), "=&v"(gw1), "=&v"(gb)
                   : "v"(o1), "v"(o2), "v"(o3), "s"(rw)
                   : "memory");
    };
    const int xin = opqs((int)WS_X + (((r * 4 + j) * 4 + g) * 4 + rg) * 6144);
    u32x4 none;
    auto side = [&](auto Nn) AI {
      constexpr int n = decltype(Nn)::v;
      if constexpr (n == 3) {
        none = u32x4{~0u, ~0u, ~0u, ~0u};
        pin(none);
      }
      if constexpr (n >= 6 && n <= 16 && !(n & 1)) stg4<((n - 6) * 512) & 4095>(none, l16, rw, xin + (((n - 6) * 512) & 4096));
      if constexpr (n == 23) stg2<0>(u32x2{tlo, thi}, lane == 0 ? (int)WS_FI + (((r * 4 + j) * 4 + rg) * 4 + g) * 128 : OOB, rw);
      if constexpr (n == 1) {
        u32 val = 0;
        int o = OOB;
        if (w == 2) {
          const int a = opq(MI_PW + lane * 4);
          float c[8];
#pragma unroll
          for (int i = 0; i < 8; ++i) c[i] = LDS(float, a + i * 256);
          o = (blk < 56 ? (int)WS_GW + ((blk & 7) * 1792 + (7 * g + (blk >> 3)) * 64) * 8 : OOB) + lane * 8;
          val = asu(((c[0] + c[1]) + (c[2] + c[3])) + ((c[4] + c[5]) + (c[6] + c[7])));
        } else if (w == 3) {
          const f32x4 p = LDS(f32x4, MI_PS + pos * 16);
          const int oa = opqs((int)WS_GS + (r * 128 + s * 8) * 8), oc = opqs((int)WS_GB + (s * 32 + r * 2) * 16 - 64);
          o = (lane < 8 ? oa : (lane < 12 ? oc : OOB)) + lane * 8;
          const float m = p.x * (1.0f / 512.0f), v = fmaxf(fmaf(-m, m, p.z * (1.0f / 512.0f)), 0.f);
          val = lane < 8 ? pk2(m, __builtin_amdgcn_sqrtf(rbf(rbf(v) + 1e-5f))) : split2(p.y);
        }
        stg2<0>(u32x2{val, tlo}, o, rw);
      } else if constexpr (n == 4) {
        o1 = opq((int)WS_GS + (r * 128 + rg * 32 + l31) * 8);
        o2 = opq((int)WS_GW + (rl * 1792 + ctc + pos * 4) * 8);
        o3 = opq((int)WS_GB + (s * 32 + l31) * 16);
      } else if constexpr (n == 5) {
        vw2 = opq(gi * 1024 + (((lane & 15) ^ ((gi ^ g) << 2)) << 4));
        a8 = opq(MI_CT + l31 * 8);
        cvo = opq(lane < (w == 3 ? 32 : 0) ? l16 : OOB);
      } else if constexpr (n == 7) {
        aw = opq(MI_CT + rg * 256 + l31 * 8 + h * 4);
        at = opq(trb + l31 * 72 + h * 8);
      } else if constexpr (n == 8) {
        lo0 = opq(d0 >= 0 ? d0 * 512 + l31 * 16 : OOB);
        lo1 = opq(d0 >= 2 ? (d0 - 2) * 512 + l31 * 16 : OOB);
      } else if constexpr (n == 9) {
        const bool ln = lane < 14;
        const int oa = (g * 3584 + blk * 56 + rg * 14 + lane) * 128, oc = (blk * 8 + (rg >> 1) * 4 + (pos >> 1)) * 1024 + g * 256 + (pos & 1) * 128;
        const int o = ln ? oa : oc;
        const u64 p2 = (u64)w2, p3 = csrc;
        tch = (const char*)(ln ? p2 : p3) + o;
        pin(tch);
      } else if constexpr (n == 11) {
        const u32 nd = (0xCFFFFu & ~(0x11111u << j)) | (0xF00000u & ~(0x100000u << g));
        const int fi = opqs((int)WS_FI + ((r * 4 + j) * 4 + rg) * 512 - 2560), ff = opqs(ffb);
        fo = opq((((nd >> l31) & 1) > h) ? (lane < 20 ? ff : fi) + lane * 128 : OOB);
      } else if constexpr (n == 17) {
        gather();
      } else if constexpr (n == 21) {
        touch(tch, MI_JUNK);
      } else if constexpr (n == 22) {
        pin(gs, gw0, gw1, gb);
        _Pragma("nounroll") for (u32 it = 0; it < SPIN_CAP; ++it) {
          const u32 d = ((gs.y ^ tlo) | (gw0.y ^ tlo)) | ((gw0.w ^ tlo) | (gw1.y ^ tlo)) | ((gw1.w ^ tlo) | (gb.y ^ tlo)) | (gb.w ^ tlo);
          if (__builtin_expect(__builtin_amdgcn_ballot_w64(d != 0) == 0, 1)) break;
          gather();
          asm volatile("s_waitcnt vmcnt(0)" : "+v"(gs), "+v"(gw0), "+v"(gw1), "+v"(gb)::"memory");
        }
      } else if constexpr (n == 23) {
        t0 = dppadd<0x128>(add16(add32(asf(gw0.x), asf(gw1.x)), add32(asf(gw0.z), asf(gw1.z))));
        pin(t0);
      } else if constexpr (n == 24) {
        const u32 p = split2(t0);
        LDS(u32x2, MI_CT + (rg * 32 + pos * 4 + gi) * 8) = u32x2{pd | (p << 16), p >> 16};
      } else if constexpr (n == 25) {
        t0 = bfh(gs.x);
        t1 = __builtin_amdgcn_rcpf(t0);
        nm = (gs.x & 0xffffu) ^ 0x8000u;
        t1 = fmaf(fmaf(-t0, t1, 1.0f), t1, t1);
        pin(t1, nm);
      } else if constexpr (n == 26) {
        const float gc0 = -2.302208198f * t1;
        LDS(u32x4, MI_RC + (rg * 32 + l31) * 16) = u32x4{asu(gc0), asu(gc0 * 0.044715f * (t1 * t1)), (gs.x >> 16) | (nm << 16), nm};
      } else if constexpr (n == 27) {
        LDS(float, MI_RS + h * 512 + (rg * 32 + l31) * 4) = h ? bf2f(nm) * t1 : t1;
        nm = h ? 0u : nm | (nm << 16);
        pin(nm);
      } else if constexpr (n == 28) {
        are = MFMA32(BC(bf16x8, (u32x4{nm, 0u, 0u, 0u})), BC(bf16x8, (u32x4{gb.x, 0u, 0u, 0u})), are); pin(are);
        aim = MFMA32(BC(bf16x8, (u32x4{nm, 0u, 0u, 0u})), BC(bf16x8, (u32x4{gb.z, 0u, 0u, 0u})), aim); pin(aim);
      } else if constexpr (n == 29) {
        const int a = opq(MI_RS + (rg * 32 + h * 4) * 4);
#pragma unroll
        for (int q = 0; q < 4; ++q) {
          rsv[q] = LDS(f32x4, a + q * 32);
          nmv[q] = LDS(f32x4, a + 512 + q * 32);
        }
      }
    };
    SB;
    bar<4>();
    STAMP(3);
    hold(3);
    rdi(0); rdx(0); rdi(1); rdx(1);
    sfor<0, 32>([&](auto Nn) AI {
      constexpr int n = decltype(Nn)::v, c = n >> 2, ks = n & 3;
      SB;
      are = MFMA32(fxr[n % 3], fbr[n % 3], are); pin(are);
      SB;
      if (ks != 2 && n + 2 < 32) rdi(n + 2);
      if constexpr (n < 2) PIECE(2, 2 * n);
      if constexpr (ks != 2) {
        if constexpr (n < 2) PIECE(2, 2 * n + 1);
        else if constexpr (n < 22) PIECE(((n - 2) >> 2) + 3, (n - 2) & 3);
        else if constexpr (n < 30) w1p(IC<n - 22>{});
        else w2t(IC<1>{});
      }
      SB;
      aim = MFMA32(fxr[n % 3], fbi[n % 3], aim); pin(aim);
      SB;
      if constexpr (ks == 2) {
        bar<abar(n)>();
        if (n + 2 < 32) rdi(n + 2);
      }
      if (n + 2 < 32) rdx(n + 2);
      if constexpr (ks == 2) {
        if constexpr (n < 22) PIECE(((n - 2) >> 2) + 3, 0);
        else if constexpr (n < 30) w1p(IC<n - 22>{});
        else w2t(IC<0>{});
      }
      SB;
      side(Nn);
    });
    SB;
    STAMP(4);
    hold(4);
    PRIO(2);
    pin(rsv[0], rsv[1], rsv[2], rsv[3]);
    cf u[16], sq[4];
#pragma unroll
    for (int i = 0; i < 16; ++i) {
      float a = are[i] * rsv[i >> 2][i & 3], c = aim[i] * rsv[i >> 2][i & 3];
      pin(a, c);
      u[i] = cf{a, c};
    }
    {
#pragma unroll
      for (int k = 1; k < 4; ++k)
#pragma unroll
        for (int q = 0; q < 4; ++q) u[4 * q + k] = cfma(l1, u[4 * q + k - 1], u[4 * q + k]);
      cf S = {0.f, 0.f};
#pragma unroll
      for (int q = 0; q < 4; ++q) {
        float ar, ai, cr, ci;
        halves(u[4 * q + 3].x, ar, cr);
        halves(u[4 * q + 3].y, ai, ci);
        const cf Sa = S;
        S = q ? cfma(l4, S, cf{ar, ai}) : cf{ar, ai};
        sq[q] = csel(h, S, Sa);
        S = cfma(l4, S, cf{cr, ci});
      }
      LDS(f32x2, a8 + (MI_WT - MI_CT) + rg * 256) = S;
    }
    cf t4[4];
    auto meet = [&](auto Oo, int a) AI {
      constexpr int o = decltype(Oo)::v;
      sig<o>(sv);
      _Pragma("nounroll") for (u32 it = 0; it < (SPIN_CAP << 4); ++it) {
        const u32 c = peek<o>(sv);
#pragma unroll
        for (int i = 0; i < 4; ++i) t4[i] = *(volatile LAS f32x2*)(size_t)(a + i * 256);
        if ((u32)__builtin_amdgcn_readfirstlane(c) >= 4u) break;
      }
    };
    meet(IC<C_WT>{}, a8 + (MI_WT - MI_CT));
    cf lin;
    {
      const cf s1 = cfma(l32, t4[0], t4[1]), s2 = cfma(l32, s1, t4[2]), s3 = cfma(l32, s2, t4[3]);
      stg4<0>(u32x4{asu(s3.x), tlo, asu(s3.y), tlo}, cvo, rw, gro);
      PRIO(0);
      lin = csel(rg & 2, csel(rg & 1, s2, s1), csel(rg & 1, t4[0], cf{0.f, 0.f}));
    }
    STAMP(5);
    waitv<5>();
    sig<C_U1>(sv);
    ctile(IC<0>{}); ctile(IC<1>{});
    PRIO(2);
    pin(co2);
#pragma unroll
    for (int i = 0; i < 16; ++i) u[i] = cmul(co2, u[i]);
#pragma unroll
    for (int q = 0; q < 4; ++q) sq[q] = cmul(co2, sq[q]);
#pragma unroll
    for (int q = 0; q < 4; ++q) pin(u[4 * q], u[4 * q + 1], u[4 * q + 2], u[4 * q + 3], sq[q]);
    pin(lin);
    PRIO(0);
    ctile(IC<2>{}); ctile(IC<3>{});
    w2t(IC<2>{}); w2t(IC<3>{}); w2t(IC<4>{});
    u32x4 lb0, lb1;
    ldg4(lb0, lo0, rw, grs);
    ldg4(lb1, lo1, rw, grs);
    lwait<C_P1, 4>(sv);
    w2t(IC<5>{}); w2t(IC<6>{}); w2t(IC<7>{}); w2t(IC<8>{});
    STAMP(6);
    asm volatile("s_waitcnt vmcnt(8)" : "+v"(lb0), "+v"(lb1)::"memory");
    sig<C_LA>(sv);
    PRIO(2);
    _Pragma("nounroll") for (u32 it = 0; it < SPIN_CAP; ++it) {
      const bool bad = ((d0 >= 0) & (((lb0.y ^ tlo) | (lb0.w ^ tlo)) != 0)) | ((d0 >= 2) & (((lb1.y ^ tlo) | (lb1.w ^ tlo)) != 0));
      if (__builtin_amdgcn_ballot_w64(bad) == 0) break;
      ldg4(lb0, lo0, rw, grs);
      ldg4(lb1, lo1, rw, grs);
      asm volatile("s_waitcnt vmcnt(0)" : "+v"(lb0), "+v"(lb1)::"memory");
    }
    {
      cf a = cmul(lF, cfma(l256, cf{asf(lb1.x), asf(lb1.z)}, cf{asf(lb0.x), asf(lb0.z)}));
      settle(a);
      LDS(float, aw) = add32(a.x, a.y);
    }
    meet(IC<C_HP>{}, a8);
    STAMP(7);
    {
      float zr = (t4[0].x + t4[1].x) + (t4[2].x + t4[3].x);
      pin(zr);
      const cf z = cfma(lL, cf{zr, (t4[0].y + t4[1].y) + (t4[2].y + t4[3].y)}, lin);
#pragma unroll
      for (int q = 0; q < 4; ++q) {
        const cf t = cfma(mq[q], z, sq[q]);
        cf v[4];
#pragma unroll
        for (int k = 0; k < 4; ++k) v[k] = cfma(k == 0 ? l1 : (k == 1 ? l2 : (k == 2 ? l3 : l4)), t, u[4 * q + k]);
        LDS(u32x2, at + 16 * q) = u32x2{pk2(v[0].x, v[1].x), pk2(v[2].x, v[3].x)};
        LDS(u32x2, at + 2304 + 16 * q) = u32x2{pk2(-v[0].y, -v[1].y), pk2(-v[2].y, -v[3].y)};
      }
    }
    sig<C_PAIR>(svp);
    PRIO(0);
    STAMP(8);
    u32x2 pv;
    int prog = 0;
    auto seen = [&]() AI {
      pin(pv);
      const bool bad = (fo != OOB) & (((pv.x ^ tlo) | (pv.y ^ thi)) != 0);
      fo = bad ? fo : OOB;
      prog = __builtin_ctzll(__builtin_amdgcn_ballot_w64(bad) | (1ull << 24)) >> 2;
      *(volatile LAS u32*)(size_t)(svp + C_PAIR + 4) = (u32)prog;
    };
    auto watch = [&](auto Oo) AI {
      constexpr int o = decltype(Oo)::v;
      _Pragma("nounroll") for (u32 it = 0; it < SPIN_CAP; ++it) {
        if constexpr (o >= 0) {
          if ((u32)__builtin_amdgcn_readfirstlane(peek<o>(sv)) >= 4u) break;
        } else {
          if (prog == 6) break;
        }
        ldg2(pv, fo, rw);
        waitv<0>();
        seen();
      }
    };
    watch(IC<C_P2>{});
    ldg2(pv, fo, rw);
    ctile(IC<4>{}); ctile(IC<5>{}); ctile(IC<6>{}); ctile(IC<7>{});
    waitv<8>();
    sig<C_LB>(sv);
    seen();
    ldg2(pv, fo, rw);
    waitv<0>();
    sig<C_LC>(sv);
    seen();
    STAMP(9);
    watch(IC<C_P3>{});
    STAMP(10);
    ldg2(pv, fo, rw);
    w2t(IC<9>{}); w2t(IC<10>{}); w2t(IC<11>{}); w2t(IC<12>{}); w2t(IC<13>{});
    waitv<10>();
    seen();
    ldg2(pv, fo, rw);
    float ep[16];
    {
      const int col = j * 128 + g * 32 + l31;
      const GAS u16* xp = (const GAS u16*)x + (size_t)(r * 128 + rg * 32 + h * 4) * 512 + col;
      u32 xv[16], dv = ((const GAS u16*)dsk)[col], bv = ((const GAS u16*)b2)[col];
#pragma unroll
      for (int e = 0; e < 16; ++e) xv[e] = xp[((e & 3) + 8 * (e >> 2)) * 512];
      SB;
      waitv<18>();
      sig<C_LD>(sv);
      seen();
      SB;
#pragma unroll
      for (int e = 0; e < 16; ++e) {
        float xf = bf2f(xv[e]), xn = fmaf(xf, rsv[e >> 2][e & 3], nmv[e >> 2][e & 3]);
        pin(xn);
        xf += bf2f(bv);
        pin(xf);
        ep[e] = fmaf(xn, bf2f(dv), xf);
        pin(ep[e]);
      }
    }
    watch(IC<-1>{});
    STAMP(11);
    lwait<C_PAIR + 8, 1>(svp);
    STAMP(12);
    u32x4 xi[6];
    _Pragma("nounroll") for (u32 it = 0; it < SPIN_CAP; ++it) {
#pragma unroll
      for (int i = 0; i < 6; ++i) ldg4(xi[i], l16, rw, xin + i * 1024);
      asm volatile("s_waitcnt vmcnt(0)" : "+v"(xi[0]), "+v"(xi[1]), "+v"(xi[2]), "+v"(xi[3]), "+v"(xi[4]), "+v"(xi[5])::"memory");
      u32 m = 0;
#pragma unroll
      for (int i = 0; i < 6; ++i) m = max(max(max(max(m, xi[i].x), xi[i].y), xi[i].z), xi[i].w);
      if (__builtin_amdgcn_ballot_w64(m == ~0u) == 0) break;
    }
    STAMP(13);
#pragma unroll
    for (int i = 0; i < 6; ++i)
#pragma unroll
      for (int e = 0; e < 4; ++e) {
        ep[(i & 1) * 8 + 2 * e] += bf2f(xi[i][e]);
        ep[(i & 1) * 8 + 2 * e + 1] += bfh(xi[i][e]);
      }
#pragma unroll
    for (int e = 0; e < 16; ++e) pin(ep[e]);
    lwait<C_PAIR + 8, 2>(svp);
    STAMP(14);
    hold(14);
    GAS float* op = (GAS float*)out + (size_t)(r * 128 + rg * 32 + h * 4) * 512 + j * 128 + g * 32 + l31;
#pragma unroll
    for (int q = 0; q < 4; ++q) {
      const f32x4 o = LDS(f32x4, trb + q * 1024 + l16);
#pragma unroll
      for (int k = 0; k < 4; ++k) op[(8 * q + k) * 512] = ep[4 * q + k] + o[k];
    }
    if (r == 15 && s == 0 && w == 0) stg2<0>(u32x2{tlo, thi}, lane == 0 ? 0 : OOB, rw);
    done();
  } else {
    stats();
    f32x16 a0[2] = {}, a1[2];
    bf16x8 xr[32], fw[3][2];
    int wa[2];
    {
      const int aw0 = (8 * h + tq) * 128 + ((tq >> 1) << 6) + (gi & 1) * 32 + tp * 8;
      wa[0] = opq(aw0 + WB);
      wa[1] = opq((aw0 ^ 64) + WB);
    }
    auto rdw = [&](int n, int t) AI {
      if (n == 32) wa[t] = opq((wa[t] + 16384) ^ (j == 1 ? 64 : 0));
      fw[n % 3][t] = ldtr(wa[t] + (n < 32 ? RO[(n >> 2) % 3] + 8192 : (U1S[(n >> 2) & 7] - 8) * 8192) + (n & 3) * 2048, 512);
    };
    auto rdx = [&](int n) AI { xr[n] = ld128(xa[n & 3] + RO[(n >> 2) % 3]); };
    u32x4 rc;
    float sd;
    u32x2 ct[4];
    u32x4 hf[44];
    float gz, gr, go;
    auto gelu = [&](const f32x16* a, int t, int o) AI {
      const int v = t >> 1;
      if (!(t & 1)) {
        if (v < 32) {
          const float x_ = a[v >> 4][v & 15];
          gz = __builtin_amdgcn_exp2f(fmaf(asf(rc.y), x_ * x_, asf(rc.x)) * x_);
          pin(gz);
        }
      } else {
        if (v < 32) gz = fmaf(gz, sd, sd);
        if (v > 0) {
          const float y = a[(v - 1) >> 4][(v - 1) & 15] * gr;
          if (v & 1) {
            go = y;
            pin(go);
          } else {
            u32 p = pk2(go, y);
            pin(p);
            const int i = (v - 2) >> 1, f = i >> 2;
            hf[o ? (f < 2 ? KT + f : 40 + f) : f][i & 3] = p;
          }
        }
        if (v < 32) {
          gr = __builtin_amdgcn_rcpf(gz);
          pin(gr);
        }
      }
    };
    const int hfo = (int)WS_HF + (r * 4 + rg) * 56 * 2048;
    const int hq = hfo + g * 14 * 2048, hso = (int)WS_HS + (r * 4 + rg) * 65536;
    const int fo = opq(lane == 0 ? ffb + j * 128 : OOB);
    SB;
    bar<4>();
    STAMP(3);
    hold(3);
    rdw(0, 1); rdw(0, 0); rdx(0); rdw(1, 1); rdw(1, 0); rdx(1);
    sfor<0, 32>([&](auto Nn) AI {
      constexpr int n = decltype(Nn)::v, ks = n & 3;
      SB;
      a0[0] = MFMA32(fw[n % 3][0], xr[n], a0[0]); pin(a0[0]);
      SB;
      if (ks != 2) {
        rdw(n + 2, 1);
        rdw(n + 2, 0);
      }
      if constexpr (n < 2) PIECE(2, 2 * n);
      SB;
      a0[1] = MFMA32(fw[n % 3][1], xr[n], a0[1]); pin(a0[1]);
      SB;
      if (ks == 2) {
        bar<4>();
        STS(4, 5 + (n >> 2));
        if (n == 30) {
          rc = LDS(u32x4, MI_RC + (rg * 32 + l31) * 16);
          const int a = opq(MI_CT + l31 * 8);
#pragma unroll
          for (int t = 0; t < 4; ++t) ct[t] = LDS(u32x2, a + t * 256);
        }
        rdw(n + 2, 1);
        rdw(n + 2, 0);
      }
      if (n + 2 < 32) rdx(n + 2);
      if constexpr (n < 2) PIECE(2, 2 * n + 1);
      else if constexpr (n < 22) PIECE(((n - 2) >> 2) + 3, (n - 2) & 3);
      else if constexpr (n < 30) w1p(IC<n - 22>{});
    });
    SB;
    PRIO(1);
    {
      const bf16x8 cb = BC(bf16x8, (u32x4{h ? 0u : rc.z, h ? 0u : rc.w, 0u, 0u}));
      sd = bf2f(rc.z);
      a0[0] = MFMA32(BC(bf16x8, (u32x4{ct[0].x, ct[0].y, ct[1].x, ct[1].y})), cb, a0[0]); pin(a0[0]);
      a1[0] = MFMA32(BC(bf16x8, (u32x4{ct[2].x, ct[2].y, ct[3].x, ct[3].y})), cb, f32x16{}); pin(a1[0]);
      const bf16x8 cc = BC(bf16x8, (u32x4{0u, 0u, h ? 0u : rc.z, h ? 0u : rc.w}));
      a0[1] = MFMA32(BC(bf16x8, (u32x4{ct[0].x, ct[0].y, ct[1].x, ct[1].y})), cc, a0[1]); pin(a0[1]);
      a1[1] = MFMA32(BC(bf16x8, (u32x4{ct[2].x, ct[2].y, ct[3].x, ct[3].y})), cc, f32x16{}); pin(a1[1]);
    }
    STAMP(4);
    hold(4);
    f32x16 acc[4] = {};
    bf16x8 wf[2][4];
    u32 pk = 0, pq = 0;
    int a2[2][4], ac[4], ta;
    auto rd2 = [&](int k, int i, int buf) AI {
      if ((k >= KS && k < KS + 4) || (k >= 22 && k < 34)) {
        const int part = k < 10 ? (k - KS) >> 1 : (k < 26 ? (k - 22) >> 1 : 2 + ((k - 26) >> 2));
        const int q = k < 10 ? k & 1 : (k < 26 ? 2 + (k & 1) : (k - 26) & 3);
        wf[buf][i] = ld128(ac[q] + part * 16384 + i * 4096);
      } else {
        const int p = k < 4 ? k >> 1 : (k < 10 ? 2 : (k < 22 ? (k - 4) >> 1 : (k - 16) >> 1));
        const int sl = W2S[p];
        wf[buf][i] = ldtr(a2[sl >= 12][i] + (sl >= 12 ? sl - 12 : sl) * 8192 + (k & 1) * 4096, 2048);
      }
    };
    auto event = [&](auto Ee) AI {
      constexpr int idx = decltype(Ee)::v, kind = EV.e[idx].kind, a = EV.e[idx].arg;
      if constexpr (kind == E_ST) {
        if constexpr (a < 4) stg4<a * 1024>(hf[a], l16, rw, hfo + u0 * 4096);
        else if constexpr (a < 6) stg4<(a - 4) * 1024>(hf[KT + a - 4], l16, rw, hfo + u1 * 4096 + (j == 1 ? 2048 : 0));
        else stg4<(a - 4) * 1024>(hf[36 + a], l16 | (j >= 2 ? 0 : OOB), rw, hfo + u1 * 4096);
      } else if constexpr (kind == E_SS) {
        stg4<a * 1024>(hf[KS + a], l16, rw, hso + s * 4096);
      } else if constexpr (kind == E_FL) {
        waitv<idx - flaglast(a) - 1>();
        stg2<a * 512>(u32x2{tlo, thi}, fo, rw);
      } else if constexpr (a >= 22 && a < 34) {
        constexpr int d = a < 26 ? 1 : 2 + (((a - 26) >> 1) & 1), part = a < 26 ? (a - 22) >> 1 : (a - 26) >> 2;
        ldg4o<part * 2048 + (a & 1) * 1024>(hf[a], l16, rw, hso + (s ^ d) * 4096);
      } else {
        ldg4o<(a & 1) * 1024>(hf[a], l16, rw, hq + tile(a < 22 ? (a - 4) >> 1 : (a - 16) >> 1) * 2048);
      }
    };
    auto hlast = [&]() AI {
      const int so = hq + 13 * 2048, jj = j;
      asm volatile("s_cmp_gt_u32 %[j], 1\n\ts_cbranch_scc1 1f\n\tbuffer_load_dwordx4 %0, %[v], %[r], %[s] offen sc1\n\t"
                   "buffer_load_dwordx4 %1, %[v], %[r], %[s] offen offset:1024 sc1\n1:"
                   : "+v"(hf[42]), "+v"(hf[43])
                   : [v] "v"(l16), [r] "s"(rw), [s] "s"(so), [j] "s"(jj)
                   : "memory", "scc");
    };
    auto fill = [&](auto Gg) AI {
      constexpr int G = decltype(Gg)::v;
#pragma unroll
      for (int t = gsub(G, GA0, GA1); t < gsub(G + 1, GA0, GA1); ++t) gelu(a0, t, 0);
#pragma unroll
      for (int t = gsub(G, GB0, GB1); t < gsub(G + 1, GB0, GB1); ++t) gelu(a1, t, 1);
      sfor<0, 5>([&](auto Cc) AI {
        constexpr int c = decltype(Cc)::v;
        if constexpr (G == chkgap(c)) chk<C_PAIR + 4, c + 1>(svp, pq);
        if constexpr (G == chkgap(c) - 1) pq = peek<C_PAIR + 4>(svp);
      });
      sfor<evfirst(G), evfirst(G + 1)>(event);
      if constexpr (G == chkgap(4) + 4) hlast();
    };
    sfor<32, 64>([&](auto Nn) AI {
      constexpr int n = decltype(Nn)::v, m = n - 32;
      SB;
      a1[0] = MFMA32(fw[n % 3][0], xr[m], a1[0]); pin(a1[0]);
      SB;
      if (m == 14) chk<C_U1, 8>(sv, pk);
      if (n + 2 < 64) rdw(n + 2, 1);
      if (m == 30) chk<C_LA, 4>(sv, pk);
      if (m >= 30) rd2(0, 2 * (m - 30), 0);
      fill(IC<2 * m>{});
      SB;
      a1[1] = MFMA32(fw[n % 3][1], xr[m], a1[1]); pin(a1[1]);
      SB;
      if (n + 2 < 64) rdw(n + 2, 0);
      if (m >= 30) rd2(0, 2 * (m - 30) + 1, 0);
      if (m == 3) {
        waitv<0>();
        sig<C_U1>(sv);
      }
      fill(IC<2 * m + 1>{});
      if (m == 13) pk = peek<C_U1>(sv);
      if (m == 15) sig<C_P1>(sv);
      if (m == 31) sig<C_P2>(sv);
      if (m == 29) pk = peek<C_LA>(sv);
      if (m == 28) {
        const int a = (4 * h + tq) * 256 + (tq << 6) + (gi & 1) * 32 + tp * 8;
#pragma unroll
        for (int i = 0; i < 4; ++i) a2[0][i] = opq(a ^ (i << 6));
      }
      if (m == 29) {
#pragma unroll
        for (int i = 0; i < 4; ++i) a2[1][i] = opq(a2[0][i] + 98304);
      }
      if (m == 30) {
#pragma unroll
        for (int i = 0; i < 4; ++i) ac[i] = opq(ax ^ (i * 32 + 32768) ^ ((j & 1) * 64));
      }
      if (m == 31) ta = opq(trb + (8 * h + tq) * 72 + (16 * (gi & 1) + 4 * tp) * 2);
      if (m == 7) STS(1, 5);
      if (m == 15) STS(1, 6);
      if (m == 23) STS(1, 7);
    });
    STS(0, 5); STS(1, 8);
    sfor<0, 44>([&](auto Kk) AI {
      constexpr int k = decltype(Kk)::v;
      if constexpr (k >= 10 && k < 42) asm volatile("s_waitcnt vmcnt(%1)" : "+v"(hf[k]) : "n"(evfirst(gap1(k)) - evfind(E_LD, k) - 1) : "memory");
      if constexpr (k == 42) asm volatile("s_waitcnt vmcnt(0)" : "+v"(hf[42]), "+v"(hf[43])::"memory");
      sfor<(k >= KD ? 1 : 0), 4>([&](auto Ii) AI {
        constexpr int i = decltype(Ii)::v;
        SB;
        acc[i] = MFMA32(BC(bf16x8, hf[k]), wf[k & 1][i], acc[i]); pin(acc[i]);
        SB;
        if (i == 1) {
          if (k == 12) chk<C_LB, 4>(sv, pk);
          if (k == 24) chk<C_LC, 4>(sv, pk);
          if (k == 32) chk<C_LD, 4>(sv, pk);
          if (k == 43) chk<C_PAIR + 4, 6>(svp, pk);
        }
        if (k + 1 < 44 && (i > 0 || k + 1 < KD)) rd2(k + 1, i, (k + 1) & 1);
        if constexpr (k == 43 && i > 1) {
          rd2(KD + i - 2, 0, 0);
          wf[0][i - 1] = wf[0][0];
        }
        if (k == KS - 1 && i == 1) {
          chk<C_PAIR, 1>(svp, pk);
#pragma unroll
          for (int f = 0; f < 4; ++f) hf[KS + f] = BC(u32x4, ldtr(ta + (f >> 1) * 2304 + (f & 1) * 16 * 72, 4 * 72));
        }
        fill(IC<gapof(k, i)>{});
        if (k == KS - 1 && i == 0) pk = peek<C_PAIR>(svp);
        if (i == 3) {
          if (k == 11) pk = peek<C_LB>(sv);
          if (k == 23) pk = peek<C_LC>(sv);
          if (k == 31) pk = peek<C_LD>(sv);
          if (k == 42) pk = peek<C_PAIR + 4>(svp);
          if (k == 13) sig<C_P3>(sv);
        }
      });
      if (k == 3) STS(1, 9);
      if (k == 5) STS(1, 10);
      if (k == 7) STS(1, 11);
      if (k == 9) STS(1, 12);
      if (k == 10) STS(1, 13);
      if (k == 12) STS(1, 14);
      if (k == 9) STS(0, 6);
      if (k == 15) STS(0, 7);
      if (k == 21) STS(0, 8);
      if (k == 27) STS(0, 9);
      if (k == 33) STS(0, 10);
      if (k == 39) STS(0, 11);
    });
    STS(0, 12);
    u32x4 xo;
    sfor<KD, 44>([&](auto Kk) AI {
      constexpr int k = decltype(Kk)::v, t = k - KD;
      SB;
      acc[0] = MFMA32(BC(bf16x8, hf[k]), wf[0][1 + t % 3], acc[0]); pin(acc[0]);
      SB;
      if (k + 2 < 44) {
        rd2(k + 2, 0, 0);
        wf[0][1 + (t + 2) % 3] = wf[0][0];
      }
      if constexpr (t < 12) {
        constexpr int i = 1 + (t >> 2), e = (t & 3) * 4;
        u32 p0 = min(pk2(acc[i][e], acc[i][e + 1]), 0xfffffffeu), p1 = min(pk2(acc[i][e + 2], acc[i][e + 3]), 0xfffffffeu);
        pin(p0, p1);
        xo[(t & 1) * 2] = p0;
        xo[(t & 1) * 2 + 1] = p1;
        if (t & 1) stg4<((t >> 1) & 1) * 1024>(xo, l16, rw, (int)WS_X + (((r * 4 + j) * 4 + (g ^ i)) * 4 + rg) * 6144 + (i - 1) * 2048);
      }
      if (t == 11) sig<C_PAIR + 8>(svp);
    });
    SB;
    STS(0, 13);
#pragma unroll
    for (int q = 0; q < 4; ++q) LDS(f32x4, trb + q * 1024 + l16) = f32x4{acc[0][4 * q], acc[0][4 * q + 1], acc[0][4 * q + 2], acc[0][4 * q + 3]};
    sig<C_PAIR + 8>(svp);
    done();
  }
}

__global__ __launch_bounds__(512, 2) void fused_ssm_ffn(const u16* __restrict__ x, const u16* __restrict__ w1, const u16* __restrict__ bre,
                                                         const u16* __restrict__ bim, char* __restrict__ ws, const u16* __restrict__ b1,
                                                         const u16* __restrict__ lre, const u16* __restrict__ lim, const u16* __restrict__ ldt,
                                                         const u16* __restrict__ cre, const u16* __restrict__ cim, const u16* __restrict__ w2,
                                                         const u16* __restrict__ dsk, const u16* __restrict__ b2, float* __restrict__ out) {
  u64 stp[1] = {0};
  STAMP(0);
  asm volatile("s_memrealtime %0\n\ts_waitcnt lgkmcnt(0)" : "=s"(stp[0])::"memory");
  const u64 ka = (u64)__builtin_amdgcn_kernarg_segment_ptr();
  u32 j0, j1;
  asm volatile("s_load_dword %0, %2, 0\n\ts_load_dword %1, %2, 64" : "=&s"(j0), "=&s"(j1) : "s"(ka) : "memory");
  const int w = __builtin_amdgcn_readfirstlane(threadIdx.x >> 6);
  if (w < 4) role<true>(w, threadIdx.x, ka, j0, j1, stp[0]);
  else role<false>(w, threadIdx.x, ka, j0, j1, stp[0]);
}

extern "C" void kernel_launch(void* const* d_in, const int* in_sizes, int n_in, void* d_out, int out_size, void* d_ws, size_t ws_size,
                              hipStream_t stream) {
  (void)hipFuncSetAttribute((const void*)fused_ssm_ffn, hipFuncAttributeMaxDynamicSharedMemorySize, LDS_BYTES);
  fused_ssm_ffn<<<256, 512, LDS_BYTES, stream>>>((const u16*)d_in[0], (const u16*)d_in[10], (const u16*)d_in[3], (const u16*)d_in[4], (char*)d_ws,
                                                 (const u16*)d_in[11], (const u16*)d_in[1], (const u16*)d_in[2], (const u16*)d_in[7],
                                                 (const u16*)d_in[5], (const u16*)d_in[6], (const u16*)d_in[12], (const u16*)d_in[8],
                                                 (const u16*)d_in[13], (float*)d_out);
}
```

```cpp
#include <hip/hip_runtime.h>
#include <hip/hip_bf16.h>

typedef unsigned short u16;
typedef unsigned u32;
typedef unsigned long long u64;
typedef short bf16x8 __attribute__((ext_vector_type(8)));
typedef short bf16x4 __attribute__((ext_vector_type(4)));
typedef float f32x16 __attribute__((ext_vector_type(16)));
typedef float f32x4 __attribute__((ext_vector_type(4)));
typedef float f32x2 __attribute__((ext_vector_type(2)));
typedef __bf16 bf16x2n __attribute__((ext_vector_type(2)));
typedef u32 u32x4 __attribute__((ext_vector_type(4)));
typedef u32 u32x2 __attribute__((ext_vector_type(2)));
typedef int i32x4 __attribute__((ext_vector_type(4)));
#define LAS __attribute__((address_space(3)))
#define DI __device__ __forceinline__
#define AI __attribute__((always_inline))
#define SB __builtin_amdgcn_sched_barrier(0)
#define PRIO(n) do { SB; asm volatile("s_setprio " #n); SB; } while (0)
#define MFMA32(a, b, c) __builtin_amdgcn_mfma_f32_32x32x16_bf16((a), (b), (c), 0, 0, 0)
#define BC(T, v) __builtin_bit_cast(T, v)
#define SC1 16

constexpr u32 WS_FF = 4096;
constexpr u32 WS_FI = WS_FF + 256 * 2560;
constexpr u32 WS_GR = WS_FI + 131072;
constexpr u32 WS_GS = WS_GR + 131072;
constexpr u32 WS_GB = WS_GS + 16384;
constexpr u32 WS_GW = WS_GB + 8192;
constexpr u32 WS_HF = 1310720;
constexpr u32 WS_HS = WS_HF + 16 * 4 * 56 * 2048;
constexpr u32 WS_X = WS_HS + 16 * 4 * 64 * 1024;
constexpr u32 WS_END = WS_X + 16 * 4 * 4 * 4 * 6144;
static_assert(WS_GW + 8 * 1792 * 8 <= WS_HF, "ws map");

constexpr int WB = 49152;
constexpr int RO[3] = {16384, 0, 32768};
constexpr int U1S[8] = {12, 13, 14, 15, 10, 11, 8, 9};
constexpr int W2S[14] = {16, 0, 1, 2, 3, 12, 13, 14, 15, 16, 0, 1, 2, 3};
constexpr int MI_TR = 139264, MI_PW = MI_TR, MI = MI_TR + 8 * 2304;
constexpr int MI_PS = MI, MI_CT = MI + 128, MI_RC = MI_CT + 1024, MI_RS = MI_RC + 2048, MI_NM = MI_RS + 512, MI_WT = MI_NM + 512, MI_HP = MI_WT + 1024,
              MI_CNT = MI_HP, MI_JUNK = MI_CNT + 128;
constexpr int MI_HQ = MI_CT;
constexpr int LDS_BYTES = 163840;
static_assert(MI_JUNK + 256 + 128 <= LDS_BYTES, "lds map");
constexpr int C_WT = 0, C_HP = 4, C_U1 = 8, C_LA = 12, C_LB = 16, C_LC = 20, C_LD = 24, C_P1 = 28, C_P2 = 32, C_P3 = 36, C_PAIR = 64;
constexpr int OOB = 0x40000000;
constexpr u32 SPIN_CAP = 1u << 18;
#define KD 22

#define STAMP(i)
#define SET 0
#define STS(set, i) do { if (SET == (set)) STAMP(i); } while (0)

DI float asf(u32 b) { return BC(float, b); }
DI u32 asu(float f) { return BC(u32, f); }
DI float bf2f(u32 b) { return asf(b << 16); }
DI float bfh(u32 b) { return asf(b & 0xffff0000u); }
DI u32 pk2(float a, float b) {
  f32x2 v = {a, b};
  return BC(u32, __builtin_convertvector(v, bf16x2n));
}
DI float rbf(float a) { return bf2f(pk2(a, a)); }
DI i32x4 mkrs(u64 a, u32 bytes) {
  i32x4 r = {(int)(u32)a, (int)((u32)(a >> 32) & 0xffffu), (int)bytes, 0x00020000};
  return r;
}
template <int SI, int LI> DI void dma1(i32x4 rs, int v, int sb, int lw) {
  int t;
  asm volatile("s_add_i32 m0, %[lw], %[li]\n\ts_add_i32 %[t], %[sb], %[si]\n\tbuffer_load_dwordx4 %[v], %[rs], %[t] offen lds"
               : [t] "=&s"(t)
               : [v] "v"(v), [rs] "s"(rs), [sb] "s"(sb), [lw] "s"(lw), [si] "n"(SI), [li] "n"(LI)
               : "memory", "scc");
}
template <int LI> DI void dma_s(i32x4 rs, int v, int& so, int ps, int lw) {
  asm volatile("s_add_i32 m0, %[lw], %[li]\n\ts_add_i32 %[t], %[t], %[ps]\n\tbuffer_load_dwordx4 %[v], %[rs], %[t] offen lds"
               : [t] "+s"(so)
               : [v] "v"(v), [rs] "s"(rs), [ps] "s"(ps), [lw] "s"(lw), [li] "n"(LI)
               : "memory", "scc");
}
DI void touch(const void* p, int lw) {
  asm volatile("s_mov_b32 m0, %1\n\ts_nop 0\n\tglobal_load_lds_dword %0, off" ::"v"(p), "s"(lw) : "memory");
}
template <int I> struct IC { static constexpr int v = I; };
template <int A, int B, class Fn> DI void sfor(Fn&& f) {
  if constexpr (A < B) {
    f(IC<A>{});
    sfor<A + 1, B>(f);
  }
}
typedef f32x2 cf;
template <class T> DI void pin1(T& v) { asm volatile("" : "+v"(v)); }
template <class... T> DI void pin(T&... v) { (pin1(v), ...); }
DI int opq(int v) { asm volatile("" : "+v"(v)); return v; }
DI int opqs(int v) { asm volatile("" : "+s"(v)); return v; }
template <int N> DI void bar() { asm volatile("s_waitcnt vmcnt(%0) lgkmcnt(0)\n\ts_barrier" ::"n"(N) : "memory"); }
template <int N> DI void waitv() { asm volatile("s_waitcnt vmcnt(%0)" ::"n"(N) : "memory"); }
DI void waitl() { asm volatile("s_waitcnt lgkmcnt(0)" ::: "memory"); }
DI bf16x8 ld128(int a) { return *(LAS bf16x8*)(size_t)a; }
DI bf16x8 ldtr(int a, int d) {
  bf16x4 lo = __builtin_amdgcn_ds_read_tr16_b64_v4i16((LAS bf16x4*)(size_t)a);
  bf16x4 hi = __builtin_amdgcn_ds_read_tr16_b64_v4i16((LAS bf16x4*)(size_t)(a + d));
  return __builtin_shufflevector(lo, hi, 0, 1, 2, 3, 4, 5, 6, 7);
}
#define LDS(T, a) (*(LAS T*)(size_t)(a))
DI float add32(float a, float b) {
  auto r = __builtin_amdgcn_permlane32_swap(asu(a), asu(b), false, false);
  return asf(r[0]) + asf(r[1]);
}
DI float add16(float a, float b) {
  auto r = __builtin_amdgcn_permlane16_swap(asu(a), asu(b), false, false);
  return asf(r[0]) + asf(r[1]);
}
template <int C> DI float dppadd(float v) { return v + asf((u32)__builtin_amdgcn_update_dpp(0, (int)asu(v), C, 0xf, 0xf, true)); }
DI float rsum(float v) {
  v = dppadd<0xB1>(v);
  v = dppadd<0x4E>(v);
  v = dppadd<0x141>(v);
  return dppadd<0x140>(v);
}
DI void halves(float v, float& a, float& b) {
  auto r = __builtin_amdgcn_permlane32_swap(asu(v), asu(v), false, false);
  a = asf(r[0]);
  b = asf(r[1]);
}
DI u32 split2(float c) {
  return pk2(c, c - rbf(c));
}
DI float dot2(u32 a, u32 b, float c) { return __builtin_amdgcn_fdot2_f32_bf16(BC(bf16x2n, a), BC(bf16x2n, b), c, false); }
DI cf pfma(cf a, cf b, cf c) {
  asm("v_pk_fma_f32 %0, %1, %2, %0 op_sel_hi:[0,1,1]\n\tv_pk_fma_f32 %0, %1, %2, %0 op_sel:[1,1,0] op_sel_hi:[1,0,1] neg_lo:[1,0,0]"
      : "+&v"(c)
      : "v"(a), "v"(b));
  return c;
}
DI cf pmul(cf a, cf b) {
  cf t;
  asm("v_pk_mul_f32 %0, %1, %2 op_sel_hi:[0,1]\n\tv_pk_fma_f32 %0, %1, %2, %0 op_sel:[1,1,0] op_sel_hi:[1,0,1] neg_lo:[1,0,0]"
      : "=&v"(t)
      : "v"(a), "v"(b));
  return t;
}
DI cf csq(cf a) { return pmul(a, a); }
DI float sfma(float a, float b, float c) { asm("v_fma_f32 %0, %1, %2, %3" : "=v"(c) : "v"(a), "v"(b), "v"(c)); return c; }
DI cf cfma(cf a, cf b, cf c) {
  float x = c.x, y = c.y;
  asm("v_fmac_f32 %0, %2, %4\n\tv_fmac_f32 %1, %2, %5\n\tv_fma_f32 %0, -%3, %5, %0\n\tv_fmac_f32 %1, %3, %4"
      : "+&v"(x), "+&v"(y)
      : "v"(a.x), "v"(a.y), "v"(b.x), "v"(b.y));
  return cf{x, y};
}
DI cf cmul(cf a, cf b) {
  float x, y;
  asm("v_mul_f32 %0, %2, %4\n\tv_mul_f32 %1, %2, %5\n\tv_fma_f32 %0, -%3, %5, %0\n\tv_fmac_f32 %1, %3, %4"
      : "=&v"(x), "=&v"(y)
      : "v"(a.x), "v"(a.y), "v"(b.x), "v"(b.y));
  return cf{x, y};
}
DI cf csel(bool c, cf a, cf b) { return cf{c ? a.x : b.x, c ? a.y : b.y}; }
DI void settle(cf& a) { asm volatile("s_nop 1" : "+v"(a)); }
template <int O> DI void sig(int sv) { asm volatile("ds_inc_u32 %0, %0 offset:%1" ::"v"(sv), "n"(O) : "memory"); }
template <int O> DI u32 peek(int sv) { return *(volatile LAS u32*)(size_t)(sv + O); }
template <int O, int N> DI void chk(int sv, u32 v) {
  int t, c;
  asm volatile("v_readfirstlane_b32 %[t], %[v]\n\ts_cmp_ge_u32 %[t], %[n]\n\ts_cbranch_scc1 2f\n\ts_mov_b32 %[c], 0x400000\n"
               "1:\n\ts_sleep 1\n\tds_read_b32 %[v], %[a] offset:%[o]\n\ts_sub_u32 %[c], %[c], 1\n\ts_waitcnt lgkmcnt(0)\n\ts_cbranch_scc1 2f\n\t"
               "v_readfirstlane_b32 %[t], %[v]\n\ts_cmp_lt_u32 %[t], %[n]\n\ts_cbranch_scc1 1b\n2:"
               : [t] "=&s"(t), [c] "=&s"(c), [v] "+v"(v)
               : [a] "v"(sv), [o] "n"(O), [n] "n"(N)
               : "memory", "scc");
}
template <int O, int N> DI void lwait(int sv) { chk<O, N>(sv, peek<O>(sv)); }
DI void ldg4(u32x4& d, int vo, i32x4 rs, int so) {
  asm volatile("buffer_load_dwordx4 %0, %1, %2, %3 offen sc1" : "=v"(d) : "v"(vo), "s"(rs), "s"(so) : "memory");
}
template <int O> DI void ldg4o(u32x4& d, int vo, i32x4 rs, int so) {
  asm volatile("buffer_load_dwordx4 %0, %1, %2, %3 offen offset:%4 sc1" : "=v"(d) : "v"(vo), "s"(rs), "s"(so), "n"(O) : "memory");
}
DI void ldg2(u32x2& d, int vo, i32x4 rs) { asm volatile("buffer_load_dwordx2 %0, %1, %2, 0 offen sc1" : "=v"(d) : "v"(vo), "s"(rs) : "memory"); }
template <int O> DI void stg4(u32x4 d, int vo, i32x4 rs, int so) {
  asm volatile("buffer_store_dwordx4 %0, %1, %2, %3 offen offset:%4 sc1" ::"v"(d), "v"(vo), "s"(rs), "s"(so), "n"(O) : "memory");
}
template <int O> DI void stg2(u32x2 d, int vo, i32x4 rs) {
  asm volatile("buffer_store_dwordx2 %0, %1, %2, 0 offen offset:%3 sc1" ::"v"(d), "v"(vo), "s"(rs), "n"(O) : "memory");
}

constexpr int aextra(int n) { return n == 17 ? 4 : ((n == 1 || n == 21 || n == 23 || (n >= 6 && n <= 16 && !(n & 1))) ? 1 : 0); }
constexpr int abar(int nb) {
  int c = (nb >= 6 && nb != 22) ? aextra(nb - 5) : 0;
  for (int m = nb < 6 ? 0 : nb - 4; m < nb; ++m) c += (m < 2 ? 2 : 1) + aextra(m);
  return c;
}
#define KS 6
#define KT 4
#define GA0 0
#define GA1 56
#define GB0 66
#define GB1 101
#define DF 16
#define LD0 24
#define LD1 24
#define LD2 26
#define LD3 26
#define LD4 18
constexpr int gapof(int k, int i) { return 64 + (k < KD ? 4 * k + i : 4 * KD + 3 * (k - KD) + i - 1); }
constexpr int gap1(int k) { return gapof(k, k < KD ? 0 : 1); }
constexpr int gsub(int g, int g0, int g1) { return g <= g0 ? 0 : (g >= g1 ? 66 : ((g - g0) * 66 + (g1 - g0) - 1) / (g1 - g0)); }
constexpr int gdone(int t, int g0, int g1) { int g = g0; while (gsub(g + 1, g0, g1) <= t) ++g; return g; }
enum { E_ST, E_SS, E_FL, E_LD };
struct Ev { int gap, kind, arg; };
struct EvTab { Ev e[64]; int n; };
constexpr int GK[6] = {10, 16, 22, 34, 40, 42};
constexpr int LDA[5] = {LD0, LD1, LD2, LD3, LD4};
constexpr int chkgap(int g) { return gap1(GK[g]) - LDA[g]; }
constexpr EvTab mkev() {
  EvTab t{};
  int sg[8] = {};
  for (int f = 0; f < 8; ++f) {
    sg[f] = f < 4 ? gdone(16 * f + 17, GA0, GA1) : gdone(16 * (f - 4) + 17, GB0, GB1);
    t.e[t.n++] = Ev{sg[f], E_ST, f};
  }
  for (int f = 0; f < 4; ++f) t.e[t.n++] = Ev{gap1(KS) + 2 * f, E_SS, f};
  t.e[t.n++] = Ev{sg[1] + DF, E_FL, 0};
  t.e[t.n++] = Ev{sg[3] + DF, E_FL, 1};
  t.e[t.n++] = Ev{gap1(KS) + 6 + DF, E_FL, 2};
  t.e[t.n++] = Ev{sg[5] + DF, E_FL, 3};
  t.e[t.n++] = Ev{sg[7] + DF, E_FL, 4};
  for (int g = 0; g < 5; ++g)
    for (int k = GK[g]; k < GK[g + 1]; ++k) t.e[t.n++] = Ev{chkgap(g) + 2 * (k - GK[g]), E_LD, k};
  for (int a = 1; a < t.n; ++a)
    for (int c = a; c > 0 && t.e[c - 1].gap > t.e[c].gap; --c) {
      const Ev x = t.e[c];
      t.e[c] = t.e[c - 1];
      t.e[c - 1] = x;
    }
  return t;
}
constexpr EvTab EV = mkev();
constexpr int evfirst(int g) { int a = 0; while (a < EV.n && EV.e[a].gap < g) ++a; return a; }
constexpr int evfind(int kind, int arg) { int a = 0; while (EV.e[a].kind != kind || EV.e[a].arg != arg) ++a; return a; }
constexpr int flaglast(int g) { return g == 2 ? evfind(E_SS, 3) : evfind(E_ST, g < 2 ? 2 * g + 1 : 2 * g - 1); }
static_assert(gdone(33, GA0, GA1) < gap1(1) && gdone(65, GA0, GA1) < gap1(3) && GA1 <= GB0, "first unit");
static_assert(gdone(17, GB0, GB1) < gap1(KT) && gdone(33, GB0, GB1) < gap1(KT + 1) && GB0 >= 65, "second unit");
static_assert(KD >= 14 && KD <= 32, "late steps");

typedef u32 u32x16 __attribute__((ext_vector_type(16)));
typedef u32 u32x8 __attribute__((ext_vector_type(8)));
#define GAS __attribute__((address_space(1)))
template <class V> DI u64 kp(V v, int i) { return (u64)v[2 * i] | ((u64)v[2 * i + 1] << 32); }

template <bool ISA> DI void role(const int w, int tid, const u64 ka, const u32 j0, const u32 j1, const u64 tstart) {
  u32x16 k0;
  u32x8 k1;
  u32x4 k2;
  u32x2 k3;
  asm volatile("s_load_dwordx16 %0, %1, 0" : "=s"(k0) : "s"(ka) : "memory");
  if constexpr (ISA)
    asm volatile("s_load_dwordx8 %0, %3, 64\n\ts_load_dwordx4 %1, %3, 96\n\ts_load_dwordx2 %2, %3, 112" : "=&s"(k1), "=&s"(k2), "=&s"(k3) : "s"(ka) : "memory");
  if constexpr (ISA) asm volatile("; A" : "+v"(tid));
  else asm volatile("; B" : "+v"(tid));
  const int lane = tid & 63;
  const int b = blockIdx.x, xcd = b & 7, q8 = b >> 3;
  const int g = xcd & 3, r = (xcd >> 2) * 8 + (q8 >> 2), j = q8 & 3, s = 4 * g + j;
  const int u0 = 7 * g + ((0x5320 >> (4 * j)) & 15), u1 = 7 * g + ((0x6411 >> (4 * j)) & 15);
  const int rg = w & 3, h = lane >> 5, l31 = lane & 31;
  const int rl = lane >> 3, pos = lane & 7, gi = lane >> 4;
  const int blk = r * 4 + j;
  const int l16 = lane * 16;

  int ox = (r * 128 + s * 8 + w) * 1024 + l16;
  int ow = ((blk & 7) * 64 + w * 8 + rl) * 3584 + (7 * g + (blk < 56 ? blk >> 3 : 0)) * 128 + pos * 16;
  int ob = (s * 32 + r * 2 + (rg >> 1)) * 1024 + l16;
  const int ctc = ((rg >> 1) ? u1 : u0) * 64 + ((rg & 1) ^ (rg >> 1 & (j == 1))) * 32;
  int ol = (s * 32 + l31) * 2, od = (ctc + pos * 4 + gi) * 2;
  const int vxE = rl * 1024 + ((pos ^ (rl >> 1)) << 4), vxO = vxE ^ 64;
  const int vw1 = rl * 3584 + ((pos ^ ((rl & 2) << 1)) << 4);
  const bool xt = ISA || w < 6;
  int vmE = xt ? vxE : vw1, vmO = xt ? vxO : vw1;
  const int pstr = xt ? 8192 : 8 * 3584, cadv = xt ? 128 - 3 * 8192 : 40 * 3584;
  const int mb1 = ISA ? 0 : opqs(s * 32768), mb2 = ISA ? 0 : opqs((w - 6) * 32 * 3584 + u0 * 128);
  int mso = opqs((ISA ? (r * 128 + w * 32) * 1024 : (xt ? mb1 : mb2)) - cadv);
  const int lw4 = opqs(rg * 4096 + (ISA ? 0 : WB));
  const u32 msz = ISA ? 2048u * 1024u : opqs(xt ? 512u * 1024u : 512u * 3584u);
  u32x4 ax_, aw_, ab_ = {};
  u32 pa, pb, pc, pd;
  pin(ox, ow, vmE, vmO);
  if constexpr (ISA) pin(ob, ol, od);
  SB;
  if constexpr (ISA) asm volatile("s_waitcnt lgkmcnt(0)" : "+s"(k0), "+s"(k1), "+s"(k2), "+s"(k3) : "s"(j0), "s"(j1) : "memory");
  else asm volatile("s_waitcnt lgkmcnt(0)" : "+s"(k0) : "s"(j0), "s"(j1) : "memory");
  const u64 x = kp(k0, 0), w1 = kp(k0, 1), bre = kp(k0, 2), bim = kp(k0, 3), ws = kp(k0, 4), b1 = kp(k0, 5), lre = kp(k0, 6), lim = kp(k0, 7);
  const u64 ldt = kp(k1, 0), cre = kp(k1, 1), cim = kp(k1, 2), w2 = kp(k1, 3), dsk = kp(k2, 0), b2 = kp(k2, 1), out = kp(k3, 0);
  if constexpr (ISA) {
    asm volatile("global_load_ushort %0, %1, %2" : "=v"(pa) : "v"(ol), "s"(lre) : "memory");
    asm volatile("global_load_ushort %0, %1, %2" : "=v"(pb) : "v"(ol), "s"(lim) : "memory");
    asm volatile("global_load_ushort %0, %1, %2" : "=v"(pc) : "v"(ol), "s"(ldt) : "memory");
    asm volatile("global_load_ushort %0, %1, %2" : "=v"(pd) : "v"(od), "s"(b1) : "memory");
    asm volatile("global_load_dwordx4 %0, %1, %2" : "=v"(ab_) : "v"(ob), "s"((w & 1) ? bim : bre) : "memory");
  }
  asm volatile("global_load_dwordx4 %0, %1, %2" : "=v"(ax_) : "v"(ox), "s"(x) : "memory");
  asm volatile("global_load_dwordx4 %0, %1, %2" : "=v"(aw_) : "v"(ow), "s"(w1) : "memory");
  const i32x4 rm = mkrs(ISA ? x : (w == 4 ? bre : (w == 5 ? bim : w1)), msz);
#define PIECE(c, p) dma_s<RO[(c) % 3] + (p) * 1024>(rm, ((p) & 1) ? vmO : vmE, mso, (p) == 0 ? cadv : pstr, lw4)
  PIECE(0, 0); PIECE(0, 1); PIECE(0, 2); PIECE(0, 3);
  u64 tag;
  asm volatile("s_load_dwordx2 %0, %1, 0" : "=s"(tag) : "s"(ws) : "memory");
  const i32x4 rw = mkrs(ws, WS_END);
  const int sv = opq(lane == 0 ? MI_CNT : MI_JUNK + lane * 4);
  if constexpr (!ISA) LDS(u32, MI_CNT + l31 * 4) = 0u;
  const int ax = opq(l31 * 128 + (((h ^ (l31 >> 1)) & 7) << 4));
  const int tq = (lane >> 2) & 3, tp = lane & 3;
  int xa[4];
#pragma unroll
  for (int k = 0; k < 4; ++k) xa[k] = opq((ax ^ (k * 32)) + rg * 4096);
  const u32 ordl = opqs((j & 2) ? ((j & 1) ? 0x51640CBAu : 0x51A40876u) : ((j & 1) ? 0x71A60354u : 0x75A64210u));
  const u32 ordh = opqs((j & 2) ? ((j & 1) ? 0xD98327u : 0x9DC32Bu) : ((j & 1) ? 0xD9C82Bu : 0xD9C83Bu));
  auto hold = [&](int at) AI {
  };
  auto done = [&]() AI {
    hold(15);
  };
  u32 tlo, thi;
  auto stats = [&]() AI {
    asm volatile("s_waitcnt vmcnt(4) lgkmcnt(0)" : "+v"(ax_), "+v"(aw_), "+s"(tag)::"memory");
    if constexpr (ISA) pin(ab_);
    tag += 1ull;
    tlo = opqs((u32)tag);
    thi = opqs((u32)(tag >> 32));
    STAMP(1);
    PIECE(1, 0);
    {
      float sm = 0.f, sq = 0.f, bs = 0.f, y[8];
#pragma unroll
      for (int i = 0; i < 4; ++i) {
        sm = dot2(ax_[i], 0x3f803f80u, sm);
        sq = dot2(ax_[i], ax_[i], sq);
        if constexpr (ISA) bs = dot2(ab_[i], 0x3f803f80u, bs);
      }
      sm = add32(sm, sq);
      sm = add16(sm, ISA ? add32(bs, bs) : sm);
      pin(sm);
      SB;
      PIECE(1, 1);
#pragma unroll
      for (int i = 0; i < 4; ++i) {
        y[2 * i] = bf2f(aw_[i]);
        y[2 * i + 1] = bfh(aw_[i]);
      }
      LDS(float, MI_PS + w * 16 + gi * 4) = rsum(sm);
#pragma unroll
      for (int i = 0; i < 4; ++i) y[i] = add32(y[i], y[i + 4]);
      pin(y[0], y[1], y[2], y[3]);
      SB;
      PIECE(1, 2);
      const float z0 = dppadd<0x128>(add16(y[0], y[2])), z1 = dppadd<0x128>(add16(y[1], y[3]));
      LDS(f32x2, MI_PW + w * 256 + (pos * 8 + gi * 2) * 4) = f32x2{z0, z1};
      SB;
      PIECE(1, 3);
    }
    STAMP(2);
  };

  const i32x4 r1 = mkrs(w1, 512u * 3584u);
  const int w1b = w * 8 * 3584 + u1 * 128, lw1 = w * 1024;
  auto w1p = [&](auto Cc) AI {
    constexpr int c = decltype(Cc)::v;
    dma1<c * 64 * 3584, U1S[c] * 8192>(r1, vw1, w1b, lw1);
  };
  auto tile = [&](int p) AI { return (int)((opqs(p >= 8 ? ordh : ordl) >> (4 * (p & 7))) & 15); };
  const int trb = MI_TR + rg * 4608;
  const int svp = sv + rg * 16;
  const int ffb = (int)WS_FF + ((r * 4 + g) * 4 + rg) * 2560;

  if constexpr (ISA) {
    cf l1, l2, l3, l4, l32, l256, co2, lF, lL, mq[4];
    {
      asm volatile("s_waitcnt vmcnt(7)" : "+v"(pa), "+v"(pb), "+v"(pc), "+v"(pd)::"memory");
      const float lr = bf2f(pa), li = bf2f(pb), dt = rbf(expf(bf2f(pc))), er = expf(dt * lr), th = dt * li;
      const float n = __builtin_rintf(th * 0.636619772f);
      const float y = fmaf(n, 1.71512451e-15f, fmaf(n, 4.37113883e-8f, fmaf(n, -1.57079637f, th))), z = y * y;
      const float pc_ = fmaf(z * z, fmaf(z, fmaf(z, 2.44331571e-5f, -1.38873163e-3f), 4.16666457e-2f), fmaf(z, -0.5f, 1.0f));
      const float ps_ = fmaf(z * y, fmaf(z, fmaf(z, -1.95152959e-4f, 8.33216087e-3f), -1.66666546e-1f), y);
      const u32 qd = (u32)(int)n;
      float sn = asf(asu((qd & 1) ? pc_ : ps_) ^ ((qd & 2) << 30)), cs = asf(asu((qd & 1) ? ps_ : pc_) ^ (((qd + 1) & 2) << 30));
      if (__builtin_expect(__builtin_amdgcn_ballot_w64(!(fabsf(th) < 8192.f)) != 0, 0)) sincosf(th, &sn, &cs);
      l1 = cf{er * cs, er * sn};
      const float den = 2.0f / fmaf(lr, lr, li * li);
      co2 = pmul(cf{l1.x - 1.0f, l1.y}, cf{lr * den, -li * den});
      const cf one = {1.f, 0.f};
      l2 = csq(l1);
      l3 = pmul(l2, l1);
      l4 = csq(l2);
      const cf l8 = csq(l4);
      l32 = csq(csq(l8));
      const cf l64 = csq(l32), l128 = csq(l64);
      l256 = csq(l128);
      const cf l512 = csq(l256);
      lF = pmul(pmul(csel(rg & 1, l512, one), csel(rg & 2, csq(l512), one)), csel(h, l128, one));
      lL = pmul(csel(rg & 1, l32, one), csel(rg & 2, l64, one));
      mq[0] = pmul(co2, csel(h, l4, one));
      mq[1] = pmul(mq[0], l8);
      mq[2] = pmul(mq[1], l8);
      mq[3] = pmul(mq[2], l8);
      pin(l1, l2, l3, l4, l32, l256, co2, lF, lL, mq[0], mq[1], mq[2], mq[3]);
    }
    stats();
    f32x16 are = {}, aim = {};
    bf16x8 fxr[3], fbr[3], fbi[3];
    int xb[4];
#pragma unroll
    for (int k = 0; k < 4; ++k) xb[k] = opq((ax ^ (k * 32)) + WB);
    auto rdi = [&](int n) AI { fbi[n % 3] = ld128(xb[n & 3] + RO[(n >> 2) % 3] + 4096); };
    auto rdx = [&](int n) AI {
      fbr[n % 3] = ld128(xb[n & 3] + RO[(n >> 2) % 3]);
      fxr[n % 3] = ld128(xa[n & 3] + RO[(n >> 2) % 3]);
    };
    const i32x4 rcr = mkrs(cre, 512u * 1024u), rci = mkrs(cim, 512u * 1024u), r2 = mkrs(w2, 1792u * 1024u);
    const int lw2 = rg * 2048, w2b = (448 * g + rg * 8) * 1024 + j * 256;
    int vw2;
    auto w2t = [&](auto Pp) AI {
      constexpr int p = decltype(Pp)::v;
      const int sb = w2b + (tile(p) << 15);
      dma1<0, W2S[p] * 8192>(r2, vw2, sb, lw2);
      dma1<4096, W2S[p] * 8192 + 1024>(r2, vw2, sb, lw2);
    };
    const int cb0 = opqs((j * 128 + (((rg >> 1) ^ g) * 32) + (rg & 1) * 16) * 1024 + g * 256 + (j >> 1) * 128);
    auto ctile = [&](auto Pp) AI {
      constexpr int p = decltype(Pp)::v;
      const int sb = opqs(cb0) ^ ((p & 1) * 65536 + (p >> 2) * 128);
      dma1<0, (4 + p) * 8192>((p & 2) ? rci : rcr, vxE, sb, lw2);
      dma1<8192, (4 + p) * 8192 + 1024>((p & 2) ? rci : rcr, vxO, sb, lw2);
    };
    float t0, t1;
    int o1, o2, o3, a8, aw, at, lo0, lo1, fo;
    u32x2 gs;
    u32x4 gw0, gw1, gb;
    u32 nm;
    f32x4 rsv[4], nmv[4];
    const char* tch;
    const u64 csrc = (u64)((rg & 1) ? cim : cre);
    const int d0 = r - 1 - 4 * rg - h;
    const int gro = opqs((int)WS_GR + (s * 16 + r) * 512), grs = opqs((int)WS_GR + s * 8192);
    int cvo;
    auto gather = [&]() AI {
      asm volatile("buffer_load_dwordx2 %0, %4, %7, 0 offen sc1\n\tbuffer_load_dwordx4 %1, %5, %7, 0 offen sc1\n\t"
                   "buffer_load_dwordx4 %2, %5, %7, 0 offen offset:16 sc1\n\tbuffer_load_dwordx4 %3, %6, %7, 0 offen sc1"
                   : "=&v"(gs), "=&v"(gw0), "=&v"(gw1), "=&v"(gb)
                   : "v"(o1), "v"(o2), "v"(o3), "s"(rw)
                   : "memory");
    };
    const int xin = opqs((int)WS_X + (((r * 4 + j) * 4 + g) * 4 + rg) * 6144);
    u32x4 none;
    auto side = [&](auto Nn) AI {
      constexpr int n = decltype(Nn)::v;
      if constexpr (n == 3) {
        none = u32x4{~0u, ~0u, ~0u, ~0u};
        pin(none);
      }
      if constexpr (n >= 6 && n <= 16 && !(n & 1)) stg4<((n - 6) * 512) & 4095>(none, l16, rw, xin + (((n - 6) * 512) & 4096));
      if constexpr (n == 23) stg2<0>(u32x2{tlo, thi}, lane == 0 ? (int)WS_FI + (((r * 4 + j) * 4 + rg) * 4 + g) * 128 : OOB, rw);
      if constexpr (n == 1) {
        u32 val = 0;
        int o = OOB;
        if (w == 2) {
          const int a = opq(MI_PW + lane * 4);
          float c[8];
#pragma unroll
          for (int i = 0; i < 8; ++i) c[i] = LDS(float, a + i * 256);
          o = (blk < 56 ? (int)WS_GW + ((blk & 7) * 1792 + (7 * g + (blk >> 3)) * 64) * 8 : OOB) + lane * 8;
          val = asu(((c[0] + c[1]) + (c[2] + c[3])) + ((c[4] + c[5]) + (c[6] + c[7])));
        } else if (w == 3) {
          const f32x4 p = LDS(f32x4, MI_PS + pos * 16);
          const int oa = opqs((int)WS_GS + (r * 128 + s * 8) * 8), oc = opqs((int)WS_GB + (s * 32 + r * 2) * 16 - 64);
          o = (lane < 8 ? oa : (lane < 12 ? oc : OOB)) + lane * 8;
          const float m = p.x * (1.0f / 512.0f), v = fmaxf(fmaf(-m, m, p.z * (1.0f / 512.0f)), 0.f);
          val = lane < 8 ? pk2(m, __builtin_amdgcn_sqrtf(rbf(rbf(v) + 1e-5f))) : split2(p.y);
        }
        stg2<0>(u32x2{val, tlo}, o, rw);
      } else if constexpr (n == 4) {
        o1 = opq((int)WS_GS + (r * 128 + rg * 32 + l31) * 8);
        o2 = opq((int)WS_GW + (rl * 1792 + ctc + pos * 4) * 8);
        o3 = opq((int)WS_GB + (s * 32 + l31) * 16);
      } else if constexpr (n == 5) {
        vw2 = opq(gi * 1024 + (((lane & 15) ^ ((gi ^ g) << 2)) << 4));
        a8 = opq(MI_CT + l31 * 8);
        cvo = opq(lane < (w == 3 ? 32 : 0) ? l16 : OOB);
      } else if constexpr (n == 7) {
        aw = opq(MI_CT + rg * 256 + l31 * 8 + h * 4);
        at = opq(trb + l31 * 72 + h * 8);
      } else if constexpr (n == 8) {
        lo0 = opq(d0 >= 0 ? d0 * 512 + l31 * 16 : OOB);
        lo1 = opq(d0 >= 2 ? (d0 - 2) * 512 + l31 * 16 : OOB);
      } else if constexpr (n == 9) {
        const bool ln = lane < 14;
        const int oa = (g * 3584 + blk * 56 + rg * 14 + lane) * 128, oc = (blk * 8 + (rg >> 1) * 4 + (pos >> 1)) * 1024 + g * 256 + (pos & 1) * 128;
        const int o = ln ? oa : oc;
        const u64 p2 = (u64)w2, p3 = csrc;
        tch = (const char*)(ln ? p2 : p3) + o;
        pin(tch);
      } else if constexpr (n == 11) {
        const u32 nd = (0xCFFFFu & ~(0x11111u << j)) | (0xF00000u & ~(0x100000u << g));
        const int fi = opqs((int)WS_FI + ((r * 4 + j) * 4 + rg) * 512 - 2560), ff = opqs(ffb);
        fo = opq((((nd >> l31) & 1) > h) ? (lane < 20 ? ff : fi) + lane * 128 : OOB);
      } else if constexpr (n == 17) {
        gather();
      } else if constexpr (n == 21) {
        touch(tch, MI_JUNK);
      } else if constexpr (n == 22) {
        pin(gs, gw0, gw1, gb);
        _Pragma("nounroll") for (u32 it = 0; it < SPIN_CAP; ++it) {
          const u32 d = ((gs.y ^ tlo) | (gw0.y ^ tlo)) | ((gw0.w ^ tlo) | (gw1.y ^ tlo)) | ((gw1.w ^ tlo) | (gb.y ^ tlo)) | (gb.w ^ tlo);
          if (__builtin_expect(__builtin_amdgcn_ballot_w64(d != 0) == 0, 1)) break;
          gather();
          asm volatile("s_waitcnt vmcnt(0)" : "+v"(gs), "+v"(gw0), "+v"(gw1), "+v"(gb)::"memory");
        }
      } else if constexpr (n == 23) {
        t0 = dppadd<0x128>(add16(add32(asf(gw0.x), asf(gw1.x)), add32(asf(gw0.z), asf(gw1.z))));
        pin(t0);
      } else if constexpr (n == 24) {
        const u32 p = split2(t0);
        LDS(u32x2, MI_CT + (rg * 32 + pos * 4 + gi) * 8) = u32x2{pd | (p << 16), p >> 16};
      } else if constexpr (n == 25) {
        t0 = bfh(gs.x);
        t1 = __builtin_amdgcn_rcpf(t0);
        nm = (gs.x & 0xffffu) ^ 0x8000u;
        t1 = fmaf(fmaf(-t0, t1, 1.0f), t1, t1);
        pin(t1, nm);
      } else if constexpr (n == 26) {
        const float gc0 = -2.302208198f * t1;
        LDS(u32x4, MI_RC + (rg * 32 + l31) * 16) = u32x4{asu(gc0), asu(gc0 * 0.044715f * (t1 * t1)), (gs.x >> 16) | (nm << 16), nm};
      } else if constexpr (n == 27) {
        LDS(float, MI_RS + h * 512 + (rg * 32 + l31) * 4) = h ? bf2f(nm) * t1 : t1;
        nm = h ? 0u : nm | (nm << 16);
        pin(nm);
      } else if constexpr (n == 28) {
        are = MFMA32(BC(bf16x8, (u32x4{nm, 0u, 0u, 0u})), BC(bf16x8, (u32x4{gb.x, 0u, 0u, 0u})), are); pin(are);
        aim = MFMA32(BC(bf16x8, (u32x4{nm, 0u, 0u, 0u})), BC(bf16x8, (u32x4{gb.z, 0u, 0u, 0u})), aim); pin(aim);
      } else if constexpr (n == 29) {
        const int a = opq(MI_RS + (rg * 32 + h * 4) * 4);
#pragma unroll
        for (int q = 0; q < 4; ++q) {
          rsv[q] = LDS(f32x4, a + q * 32);
          nmv[q] = LDS(f32x4, a + 512 + q * 32);
        }
      }
    };
    SB;
    bar<4>();
    STAMP(3);
    hold(3);
    rdi(0); rdx(0); rdi(1); rdx(1);
    sfor<0, 32>([&](auto Nn) AI {
      constexpr int n = decltype(Nn)::v, c = n >> 2, ks = n & 3;
      SB;
      are = MFMA32(fxr[n % 3], fbr[n % 3], are); pin(are);
      SB;
      if (ks != 2 && n + 2 < 32) rdi(n + 2);
      if constexpr (n < 2) PIECE(2, 2 * n);
      if constexpr (ks != 2) {
        if constexpr (n < 2) PIECE(2, 2 * n + 1);
        else if constexpr (n < 22) PIECE(((n - 2) >> 2) + 3, (n - 2) & 3);
        else if constexpr (n < 30) w1p(IC<n - 22>{});
        else w2t(IC<1>{});
      }
      SB;
      aim = MFMA32(fxr[n % 3], fbi[n % 3], aim); pin(aim);
      SB;
      if constexpr (ks == 2) {
        bar<abar(n)>();
        if (n + 2 < 32) rdi(n + 2);
      }
      if (n + 2 < 32) rdx(n + 2);
      if constexpr (ks == 2) {
        if constexpr (n < 22) PIECE(((n - 2) >> 2) + 3, 0);
        else if constexpr (n < 30) w1p(IC<n - 22>{});
        else w2t(IC<0>{});
      }
      SB;
      side(Nn);
    });
    SB;
    STAMP(4);
    hold(4);
    PRIO(2);
    pin(rsv[0], rsv[1], rsv[2], rsv[3]);
    cf u[16], sq[4];
#pragma unroll
    for (int i = 0; i < 16; ++i) {
      float a = are[i] * rsv[i >> 2][i & 3], c = aim[i] * rsv[i >> 2][i & 3];
      pin(a, c);
      u[i] = cf{a, c};
    }
    {
#pragma unroll
      for (int k = 1; k < 4; ++k)
#pragma unroll
        for (int q = 0; q < 4; ++q) u[4 * q + k] = cfma(l1, u[4 * q + k - 1], u[4 * q + k]);
      cf S = {0.f, 0.f};
#pragma unroll
      for (int q = 0; q < 4; ++q) {
        float ar, ai, cr, ci;
        halves(u[4 * q + 3].x, ar, cr);
        halves(u[4 * q + 3].y, ai, ci);
        const cf Sa = S;
        S = q ? cfma(l4, S, cf{ar, ai}) : cf{ar, ai};
        sq[q] = csel(h, S, Sa);
        S = cfma(l4, S, cf{cr, ci});
      }
      LDS(f32x2, a8 + (MI_WT - MI_CT) + rg * 256) = S;
    }
    cf t4[4];
    auto meet = [&](auto Oo, int a) AI {
      constexpr int o = decltype(Oo)::v;
      sig<o>(sv);
      _Pragma("nounroll") for (u32 it = 0; it < (SPIN_CAP << 4); ++it) {
        const u32 c = peek<o>(sv);
#pragma unroll
        for (int i = 0; i < 4; ++i) t4[i] = *(volatile LAS f32x2*)(size_t)(a + i * 256);
        if ((u32)__builtin_amdgcn_readfirstlane(c) >= 4u) break;
      }
    };
    meet(IC<C_WT>{}, a8 + (MI_WT - MI_CT));
    cf lin;
    {
      const cf s1 = cfma(l32, t4[0], t4[1]), s2 = cfma(l32, s1, t4[2]), s3 = cfma(l32, s2, t4[3]);
      stg4<0>(u32x4{asu(s3.x), tlo, asu(s3.y), tlo}, cvo, rw, gro);
      PRIO(0);
      lin = csel(rg & 2, csel(rg & 1, s2, s1), csel(rg & 1, t4[0], cf{0.f, 0.f}));
    }
    STAMP(5);
    waitv<5>();
    sig<C_U1>(sv);
    ctile(IC<0>{}); ctile(IC<1>{});
    PRIO(2);
    pin(co2);
#pragma unroll
    for (int i = 0; i < 16; ++i) u[i] = cmul(co2, u[i]);
#pragma unroll
    for (int q = 0; q < 4; ++q) sq[q] = cmul(co2, sq[q]);
#pragma unroll
    for (int q = 0; q < 4; ++q) pin(u[4 * q], u[4 * q + 1], u[4 * q + 2], u[4 * q + 3], sq[q]);
    pin(lin);
    PRIO(0);
    ctile(IC<2>{}); ctile(IC<3>{});
    w2t(IC<2>{}); w2t(IC<3>{}); w2t(IC<4>{});
    u32x4 lb0, lb1;
    ldg4(lb0, lo0, rw, grs);
    ldg4(lb1, lo1, rw, grs);
    lwait<C_P1, 4>(sv);
    w2t(IC<5>{}); w2t(IC<6>{}); w2t(IC<7>{}); w2t(IC<8>{});
    STAMP(6);
    asm volatile("s_waitcnt vmcnt(8)" : "+v"(lb0), "+v"(lb1)::"memory");
    sig<C_LA>(sv);
    PRIO(2);
    _Pragma("nounroll") for (u32 it = 0; it < SPIN_CAP; ++it) {
      const bool bad = ((d0 >= 0) & (((lb0.y ^ tlo) | (lb0.w ^ tlo)) != 0)) | ((d0 >= 2) & (((lb1.y ^ tlo) | (lb1.w ^ tlo)) != 0));
      if (__builtin_amdgcn_ballot_w64(bad) == 0) break;
      ldg4(lb0, lo0, rw, grs);
      ldg4(lb1, lo1, rw, grs);
      asm volatile("s_waitcnt vmcnt(0)" : "+v"(lb0), "+v"(lb1)::"memory");
    }
    {
      cf a = cmul(lF, cfma(l256, cf{asf(lb1.x), asf(lb1.z)}, cf{asf(lb0.x), asf(lb0.z)}));
      settle(a);
      LDS(float, aw) = add32(a.x, a.y);
    }
    meet(IC<C_HP>{}, a8);
    STAMP(7);
    {
      float zr = (t4[0].x + t4[1].x) + (t4[2].x + t4[3].x);
      pin(zr);
      const cf z = cfma(lL, cf{zr, (t4[0].y + t4[1].y) + (t4[2].y + t4[3].y)}, lin);
#pragma unroll
      for (int q = 0; q < 4; ++q) {
        const cf t = cfma(mq[q], z, sq[q]);
        cf v[4];
#pragma unroll
        for (int k = 0; k < 4; ++k) v[k] = cfma(k == 0 ? l1 : (k == 1 ? l2 : (k == 2 ? l3 : l4)), t, u[4 * q + k]);
        LDS(u32x2, at + 16 * q) = u32x2{pk2(v[0].x, v[1].x), pk2(v[2].x, v[3].x)};
        LDS(u32x2, at + 2304 + 16 * q) = u32x2{pk2(-v[0].y, -v[1].y), pk2(-v[2].y, -v[3].y)};
      }
    }
    sig<C_PAIR>(svp);
    PRIO(0);
    STAMP(8);
    u32x2 pv;
    int prog = 0;
    auto seen = [&]() AI {
      pin(pv);
      const bool bad = (fo != OOB) & (((pv.x ^ tlo) | (pv.y ^ thi)) != 0);
      fo = bad ? fo : OOB;
      prog = __builtin_ctzll(__builtin_amdgcn_ballot_w64(bad) | (1ull << 24)) >> 2;
      *(volatile LAS u32*)(size_t)(svp + C_PAIR + 4) = (u32)prog;
    };
    auto watch = [&](auto Oo) AI {
      constexpr int o = decltype(Oo)::v;
      _Pragma("nounroll") for (u32 it = 0; it < SPIN_CAP; ++it) {
        if constexpr (o >= 0) {
          if ((u32)__builtin_amdgcn_readfirstlane(peek<o>(sv)) >= 4u) break;
        } else {
          if (prog == 6) break;
        }
        ldg2(pv, fo, rw);
        waitv<0>();
        seen();
      }
    };
    watch(IC<C_P2>{});
    ldg2(pv, fo, rw);
    ctile(IC<4>{}); ctile(IC<5>{}); ctile(IC<6>{}); ctile(IC<7>{});
    waitv<8>();
    sig<C_LB>(sv);
    seen();
    ldg2(pv, fo, rw);
    waitv<0>();
    sig<C_LC>(sv);
    seen();
    STAMP(9);
    watch(IC<C_P3>{});
    STAMP(10);
    ldg2(pv, fo, rw);
    w2t(IC<9>{}); w2t(IC<10>{}); w2t(IC<11>{}); w2t(IC<12>{}); w2t(IC<13>{});
    waitv<10>();
    seen();
    ldg2(pv, fo, rw);
    float ep[16];
    {
      const int col = j * 128 + g * 32 + l31;
      const GAS u16* xp = (const GAS u16*)x + (size_t)(r * 128 + rg * 32 + h * 4) * 512 + col;
      u32 xv[16], dv = ((const GAS u16*)dsk)[col], bv = ((const GAS u16*)b2)[col];
#pragma unroll
      for (int e = 0; e < 16; ++e) xv[e] = xp[((e & 3) + 8 * (e >> 2)) * 512];
      SB;
      waitv<18>();
      sig<C_LD>(sv);
      seen();
      SB;
#pragma unroll
      for (int e = 0; e < 16; ++e) {
        float xf = bf2f(xv[e]), xn = fmaf(xf, rsv[e >> 2][e & 3], nmv[e >> 2][e & 3]);
        pin(xn);
        xf += bf2f(bv);
        pin(xf);
        ep[e] = fmaf(xn, bf2f(dv), xf);
        pin(ep[e]);
      }
    }
    watch(IC<-1>{});
    STAMP(11);
    lwait<C_PAIR + 8, 1>(svp);
    STAMP(12);
    u32x4 xi[6];
    _Pragma("nounroll") for (u32 it = 0; it < SPIN_CAP; ++it) {
#pragma unroll
      for (int i = 0; i < 6; ++i) ldg4(xi[i], l16, rw, xin + i * 1024);
      asm volatile("s_waitcnt vmcnt(0)" : "+v"(xi[0]), "+v"(xi[1]), "+v"(xi[2]), "+v"(xi[3]), "+v"(xi[4]), "+v"(xi[5])::"memory");
      u32 m = 0;
#pragma unroll
      for (int i = 0; i < 6; ++i) m = max(max(max(max(m, xi[i].x), xi[i].y), xi[i].z), xi[i].w);
      if (__builtin_amdgcn_ballot_w64(m == ~0u) == 0) break;
    }
    STAMP(13);
#pragma unroll
    for (int i = 0; i < 6; ++i)
#pragma unroll
      for (int e = 0; e < 4; ++e) {
        ep[(i & 1) * 8 + 2 * e] += bf2f(xi[i][e]);
        ep[(i & 1) * 8 + 2 * e + 1] += bfh(xi[i][e]);
      }
#pragma unroll
    for (int e = 0; e < 16; ++e) pin(ep[e]);
    lwait<C_PAIR + 8, 2>(svp);
    STAMP(14);
    hold(14);
    GAS float* op = (GAS float*)out + (size_t)(r * 128 + rg * 32 + h * 4) * 512 + j * 128 + g * 32 + l31;
#pragma unroll
    for (int q = 0; q < 4; ++q) {
      const f32x4 o = LDS(f32x4, trb + q * 1024 + l16);
#pragma unroll
      for (int k = 0; k < 4; ++k) __hip_atomic_store(&op[(8 * q + k) * 512], ep[4 * q + k] + o[k], __ATOMIC_RELAXED, __HIP_MEMORY_SCOPE_AGENT);
    }
    if (r == 15 && s == 0 && w == 0) stg2<0>(u32x2{tlo, thi}, lane == 0 ? 0 : OOB, rw);
    done();
  } else {
    stats();
    f32x16 a0[2] = {}, a1[2];
    bf16x8 xr[32], fw[3][2];
    int wa[2];
    {
      const int aw0 = (8 * h + tq) * 128 + ((tq >> 1) << 6) + (gi & 1) * 32 + tp * 8;
      wa[0] = opq(aw0 + WB);
      wa[1] = opq((aw0 ^ 64) + WB);
    }
    auto rdw = [&](int n, int t) AI {
      if (n == 32) wa[t] = opq((wa[t] + 16384) ^ (j == 1 ? 64 : 0));
      fw[n % 3][t] = ldtr(wa[t] + (n < 32 ? RO[(n >> 2) % 3] + 8192 : (U1S[(n >> 2) & 7] - 8) * 8192) + (n & 3) * 2048, 512);
    };
    auto rdx = [&](int n) AI { xr[n] = ld128(xa[n & 3] + RO[(n >> 2) % 3]); };
    u32x4 rc;
    float sd;
    u32x2 ct[4];
    u32x4 hf[44];
    float gz, gr, go;
    auto gelu = [&](const f32x16* a, int t, int o) AI {
      const int v = t >> 1;
      if (!(t & 1)) {
        if (v < 32) {
          const float x_ = a[v >> 4][v & 15];
          gz = __builtin_amdgcn_exp2f(fmaf(asf(rc.y), x_ * x_, asf(rc.x)) * x_);
          pin(gz);
        }
      } else {
        if (v < 32) gz = fmaf(gz, sd, sd);
        if (v > 0) {
          const float y = a[(v - 1) >> 4][(v - 1) & 15] * gr;
          if (v & 1) {
            go = y;
            pin(go);
          } else {
            u32 p = pk2(go, y);
            pin(p);
            const int i = (v - 2) >> 1, f = i >> 2;
            hf[o ? (f < 2 ? KT + f : 40 + f) : f][i & 3] = p;
          }
        }
        if (v < 32) {
          gr = __builtin_amdgcn_rcpf(gz);
          pin(gr);
        }
      }
    };
    const int hfo = (int)WS_HF + (r * 4 + rg) * 56 * 2048;
    const int hq = hfo + g * 14 * 2048, hso = (int)WS_HS + (r * 4 + rg) * 65536;
    const int fo = opq(lane == 0 ? ffb + j * 128 : OOB);
    SB;
    bar<4>();
    STAMP(3);
    hold(3);
    rdw(0, 1); rdw(0, 0); rdx(0); rdw(1, 1); rdw(1, 0); rdx(1);
    sfor<0, 32>([&](auto Nn) AI {
      constexpr int n = decltype(Nn)::v, ks = n & 3;
      SB;
      a0[0] = MFMA32(fw[n % 3][0], xr[n], a0[0]); pin(a0[0]);
      SB;
      if (ks != 2) {
        rdw(n + 2, 1);
        rdw(n + 2, 0);
      }
      if constexpr (n < 2) PIECE(2, 2 * n);
      SB;
      a0[1] = MFMA32(fw[n % 3][1], xr[n], a0[1]); pin(a0[1]);
      SB;
      if (ks == 2) {
        bar<4>();
        STS(4, 5 + (n >> 2));
        if (n == 30) {
          rc = LDS(u32x4, MI_RC + (rg * 32 + l31) * 16);
          const int a = opq(MI_CT + l31 * 8);
#pragma unroll
          for (int t = 0; t < 4; ++t) ct[t] = LDS(u32x2, a + t * 256);
        }
        rdw(n + 2, 1);
        rdw(n + 2, 0);
      }
      if (n + 2 < 32) rdx(n + 2);
      if constexpr (n < 2) PIECE(2, 2 * n + 1);
      else if constexpr (n < 22) PIECE(((n - 2) >> 2) + 3, (n - 2) & 3);
      else if constexpr (n < 30) w1p(IC<n - 22>{});
    });
    SB;
    PRIO(1);
    {
      const bf16x8 cb = BC(bf16x8, (u32x4{h ? 0u : rc.z, h ? 0u : rc.w, 0u, 0u}));
      sd = bf2f(rc.z);
      a0[0] = MFMA32(BC(bf16x8, (u32x4{ct[0].x, ct[0].y, ct[1].x, ct[1].y})), cb, a0[0]); pin(a0[0]);
      a1[0] = MFMA32(BC(bf16x8, (u32x4{ct[2].x, ct[2].y, ct[3].x, ct[3].y})), cb, f32x16{}); pin(a1[0]);
      const bf16x8 cc = BC(bf16x8, (u32x4{0u, 0u, h ? 0u : rc.z, h ? 0u : rc.w}));
      a0[1] = MFMA32(BC(bf16x8, (u32x4{ct[0].x, ct[0].y, ct[1].x, ct[1].y})), cc, a0[1]); pin(a0[1]);
      a1[1] = MFMA32(BC(bf16x8, (u32x4{ct[2].x, ct[2].y, ct[3].x, ct[3].y})), cc, f32x16{}); pin(a1[1]);
    }
    STAMP(4);
    hold(4);
    f32x16 acc[4] = {};
    bf16x8 wf[2][4];
    u32 pk = 0, pq = 0;
    int a2[2][4], ac[4], ta;
    auto rd2 = [&](int k, int i, int buf) AI {
      if ((k >= KS && k < KS + 4) || (k >= 22 && k < 34)) {
        const int part = k < 10 ? (k - KS) >> 1 : (k < 26 ? (k - 22) >> 1 : 2 + ((k - 26) >> 2));
        const int q = k < 10 ? k & 1 : (k < 26 ? 2 + (k & 1) : (k - 26) & 3);
        wf[buf][i] = ld128(ac[q] + part * 16384 + i * 4096);
      } else {
        const int p = k < 4 ? k >> 1 : (k < 10 ? 2 : (k < 22 ? (k - 4) >> 1 : (k - 16) >> 1));
        const int sl = W2S[p];
        wf[buf][i] = ldtr(a2[sl >= 12][i] + (sl >= 12 ? sl - 12 : sl) * 8192 + (k & 1) * 4096, 2048);
      }
    };
    auto event = [&](auto Ee) AI {
      constexpr int idx = decltype(Ee)::v, kind = EV.e[idx].kind, a = EV.e[idx].arg;
      if constexpr (kind == E_ST) {
        if constexpr (a < 4) stg4<a * 1024>(hf[a], l16, rw, hfo + u0 * 4096);
        else if constexpr (a < 6) stg4<(a - 4) * 1024>(hf[KT + a - 4], l16, rw, hfo + u1 * 4096 + (j == 1 ? 2048 : 0));
        else stg4<(a - 4) * 1024>(hf[36 + a], l16 | (j >= 2 ? 0 : OOB), rw, hfo + u1 * 4096);
      } else if constexpr (kind == E_SS) {
        stg4<a * 1024>(hf[KS + a], l16, rw, hso + s * 4096);
      } else if constexpr (kind == E_FL) {
        waitv<idx - flaglast(a) - 1>();
        stg2<a * 512>(u32x2{tlo, thi}, fo, rw);
      } else if constexpr (a >= 22 && a < 34) {
        constexpr int d = a < 26 ? 1 : 2 + (((a - 26) >> 1) & 1), part = a < 26 ? (a - 22) >> 1 : (a - 26) >> 2;
        ldg4o<part * 2048 + (a & 1) * 1024>(hf[a], l16, rw, hso + (s ^ d) * 4096);
      } else {
        ldg4o<(a & 1) * 1024>(hf[a], l16, rw, hq + tile(a < 22 ? (a - 4) >> 1 : (a - 16) >> 1) * 2048);
      }
    };
    auto hlast = [&]() AI {
      const int so = hq + 13 * 2048, jj = j;
      asm volatile("s_cmp_gt_u32 %[j], 1\n\ts_cbranch_scc1 1f\n\tbuffer_load_dwordx4 %0, %[v], %[r], %[s] offen sc1\n\t"
                   "buffer_load_dwordx4 %1, %[v], %[r], %[s] offen offset:1024 sc1\n1:"
                   : "+v"(hf[42]), "+v"(hf[43])
                   : [v] "v"(l16), [r] "s"(rw), [s] "s"(so), [j] "s"(jj)
                   : "memory", "scc");
    };
    auto fill = [&](auto Gg) AI {
      constexpr int G = decltype(Gg)::v;
#pragma unroll
      for (int t = gsub(G, GA0, GA1); t < gsub(G + 1, GA0, GA1); ++t) gelu(a0, t, 0);
#pragma unroll
      for (int t = gsub(G, GB0, GB1); t < gsub(G + 1, GB0, GB1); ++t) gelu(a1, t, 1);
      sfor<0, 5>([&](auto Cc) AI {
        constexpr int c = decltype(Cc)::v;
        if constexpr (G == chkgap(c)) chk<C_PAIR + 4, c + 1>(svp, pq);
        if constexpr (G == chkgap(c) - 1) pq = peek<C_PAIR + 4>(svp);
      });
      sfor<evfirst(G), evfirst(G + 1)>(event);
      if constexpr (G == chkgap(4) + 4) hlast();
    };
    sfor<32, 64>([&](auto Nn) AI {
      constexpr int n = decltype(Nn)::v, m = n - 32;
      SB;
      a1[0] = MFMA32(fw[n % 3][0], xr[m], a1[0]); pin(a1[0]);
      SB;
      if (m == 14) chk<C_U1, 8>(sv, pk);
      if (n + 2 < 64) rdw(n + 2, 1);
      if (m == 30) chk<C_LA, 4>(sv, pk);
      if (m >= 30) rd2(0, 2 * (m - 30), 0);
      fill(IC<2 * m>{});
      SB;
      a1[1] = MFMA32(fw[n % 3][1], xr[m], a1[1]); pin(a1[1]);
      SB;
      if (n + 2 < 64) rdw(n + 2, 0);
      if (m >= 30) rd2(0, 2 * (m - 30) + 1, 0);
      if (m == 3) {
        waitv<0>();
        sig<C_U1>(sv);
      }
      fill(IC<2 * m + 1>{});
      if (m == 13) pk = peek<C_U1>(sv);
      if (m == 15) sig<C_P1>(sv);
      if (m == 31) sig<C_P2>(sv);
      if (m == 29) pk = peek<C_LA>(sv);
      if (m == 28) {
        const int a = (4 * h + tq) * 256 + (tq << 6) + (gi & 1) * 32 + tp * 8;
#pragma unroll
        for (int i = 0; i < 4; ++i) a2[0][i] = opq(a ^ (i << 6));
      }
      if (m == 29) {
#pragma unroll
        for (int i = 0; i < 4; ++i) a2[1][i] = opq(a2[0][i] + 98304);
      }
      if (m == 30) {
#pragma unroll
        for (int i = 0; i < 4; ++i) ac[i] = opq(ax ^ (i * 32 + 32768) ^ ((j & 1) * 64));
      }
      if (m == 31) ta = opq(trb + (8 * h + tq) * 72 + (16 * (gi & 1) + 4 * tp) * 2);
      if (m == 7) STS(1, 5);
      if (m == 15) STS(1, 6);
      if (m == 23) STS(1, 7);
    });
    STS(0, 5); STS(1, 8);
    sfor<0, 44>([&](auto Kk) AI {
      constexpr int k = decltype(Kk)::v;
      if constexpr (k >= 10 && k < 42) asm volatile("s_waitcnt vmcnt(%1)" : "+v"(hf[k]) : "n"(evfirst(gap1(k)) - evfind(E_LD, k) - 1) : "memory");
      if constexpr (k == 42) asm volatile("s_waitcnt vmcnt(0)" : "+v"(hf[42]), "+v"(hf[43])::"memory");
      sfor<(k >= KD ? 1 : 0), 4>([&](auto Ii) AI {
        constexpr int i = decltype(Ii)::v;
        SB;
        acc[i] = MFMA32(BC(bf16x8, hf[k]), wf[k & 1][i], acc[i]); pin(acc[i]);
        SB;
        if (i == 1) {
          if (k == 12) chk<C_LB, 4>(sv, pk);
          if (k == 24) chk<C_LC, 4>(sv, pk);
          if (k == 32) chk<C_LD, 4>(sv, pk);
          if (k == 43) chk<C_PAIR + 4, 6>(svp, pk);
        }
        if (k + 1 < 44 && (i > 0 || k + 1 < KD)) rd2(k + 1, i, (k + 1) & 1);
        if constexpr (k == 43 && i > 1) {
          rd2(KD + i - 2, 0, 0);
          wf[0][i - 1] = wf[0][0];
        }
        if (k == KS - 1 && i == 1) {
          chk<C_PAIR, 1>(svp, pk);
#pragma unroll
          for (int f = 0; f < 4; ++f) hf[KS + f] = BC(u32x4, ldtr(ta + (f >> 1) * 2304 + (f & 1) * 16 * 72, 4 * 72));
        }
        fill(IC<gapof(k, i)>{});
        if (k == KS - 1 && i == 0) pk = peek<C_PAIR>(svp);
        if (i == 3) {
          if (k == 11) pk = peek<C_LB>(sv);
          if (k == 23) pk = peek<C_LC>(sv);
          if (k == 31) pk = peek<C_LD>(sv);
          if (k == 42) pk = peek<C_PAIR + 4>(svp);
          if (k == 13) sig<C_P3>(sv);
        }
      });
      if (k == 3) STS(1, 9);
      if (k == 5) STS(1, 10);
      if (k == 7) STS(1, 11);
      if (k == 9) STS(1, 12);
      if (k == 10) STS(1, 13);
      if (k == 12) STS(1, 14);
      if (k == 9) STS(0, 6);
      if (k == 15) STS(0, 7);
      if (k == 21) STS(0, 8);
      if (k == 27) STS(0, 9);
      if (k == 33) STS(0, 10);
      if (k == 39) STS(0, 11);
    });
    STS(0, 12);
    u32x4 xo;
    sfor<KD, 44>([&](auto Kk) AI {
      constexpr int k = decltype(Kk)::v, t = k - KD;
      SB;
      acc[0] = MFMA32(BC(bf16x8, hf[k]), wf[0][1 + t % 3], acc[0]); pin(acc[0]);
      SB;
      if (k + 2 < 44) {
        rd2(k + 2, 0, 0);
        wf[0][1 + (t + 2) % 3] = wf[0][0];
      }
      if constexpr (t < 12) {
        constexpr int i = 1 + (t >> 2), e = (t & 3) * 4;
        u32 p0 = min(pk2(acc[i][e], acc[i][e + 1]), 0xfffffffeu), p1 = min(pk2(acc[i][e + 2], acc[i][e + 3]), 0xfffffffeu);
        pin(p0, p1);
        xo[(t & 1) * 2] = p0;
        xo[(t & 1) * 2 + 1] = p1;
        if (t & 1) stg4<((t >> 1) & 1) * 1024>(xo, l16, rw, (int)WS_X + (((r * 4 + j) * 4 + (g ^ i)) * 4 + rg) * 6144 + (i - 1) * 2048);
      }
      if (t == 11) sig<C_PAIR + 8>(svp);
    });
    SB;
    STS(0, 13);
#pragma unroll
    for (int q = 0; q < 4; ++q) LDS(f32x4, trb + q * 1024 + l16) = f32x4{acc[0][4 * q], acc[0][4 * q + 1], acc[0][4 * q + 2], acc[0][4 * q + 3]};
    sig<C_PAIR + 8>(svp);
    done();
  }
}

__global__ __launch_bounds__(512, 2) void fused_ssm_ffn(const u16* __restrict__ x, const u16* __restrict__ w1, const u16* __restrict__ bre,
                                                         const u16* __restrict__ bim, char* __restrict__ ws, const u16* __restrict__ b1,
                                                         const u16* __restrict__ lre, const u16* __restrict__ lim, const u16* __restrict__ ldt,
                                                         const u16* __restrict__ cre, const u16* __restrict__ cim, const u16* __restrict__ w2,
                                                         const u16* __restrict__ dsk, const u16* __restrict__ b2, float* __restrict__ out) {
  u64 stp[1] = {0};
  STAMP(0);
  const u64 ka = (u64)__builtin_amdgcn_kernarg_segment_ptr();
  u32 j0, j1;
  asm volatile("s_load_dword %0, %2, 0\n\ts_load_dword %1, %2, 64" : "=&s"(j0), "=&s"(j1) : "s"(ka) : "memory");
  const int w = __builtin_amdgcn_readfirstlane(threadIdx.x >> 6);
  if (w < 4) role<true>(w, threadIdx.x, ka, j0, j1, stp[0]);
  else role<false>(w, threadIdx.x, ka, j0, j1, stp[0]);
}

extern "C" void kernel_launch(void* const* d_in, const int* in_sizes, int n_in, void* d_out, int out_size, void* d_ws, size_t ws_size,
                              hipStream_t stream) {
  (void)hipFuncSetAttribute((const void*)fused_ssm_ffn, hipFuncAttributeMaxDynamicSharedMemorySize, LDS_BYTES);
  fused_ssm_ffn<<<256, 512, LDS_BYTES, stream>>>((const u16*)d_in[0], (const u16*)d_in[10], (const u16*)d_in[3], (const u16*)d_in[4], (char*)d_ws,
                                                 (const u16*)d_in[11], (const u16*)d_in[1], (const u16*)d_in[2], (const u16*)d_in[7],
                                                 (const u16*)d_in[5], (const u16*)d_in[6], (const u16*)d_in[12], (const u16*)d_in[8],
                                                 (const u16*)d_in[13], (float*)d_out);
}
```

```cpp
#define HOLD 2500
#define HOLDAT 15
#include <hip/hip_runtime.h>
#include <hip/hip_bf16.h>

typedef unsigned short u16;
typedef unsigned u32;
typedef unsigned long long u64;
typedef short bf16x8 __attribute__((ext_vector_type(8)));
typedef short bf16x4 __attribute__((ext_vector_type(4)));
typedef float f32x16 __attribute__((ext_vector_type(16)));
typedef float f32x4 __attribute__((ext_vector_type(4)));
typedef float f32x2 __attribute__((ext_vector_type(2)));
typedef __bf16 bf16x2n __attribute__((ext_vector_type(2)));
typedef u32 u32x4 __attribute__((ext_vector_type(4)));
typedef u32 u32x2 __attribute__((ext_vector_type(2)));
typedef int i32x4 __attribute__((ext_vector_type(4)));
#define LAS __attribute__((address_space(3)))
#define DI __device__ __forceinline__
#define AI __attribute__((always_inline))
#define SB __builtin_amdgcn_sched_barrier(0)
#define PRIO(n) do { SB; asm volatile("s_setprio " #n); SB; } while (0)
#define MFMA32(a, b, c) __builtin_amdgcn_mfma_f32_32x32x16_bf16((a), (b), (c), 0, 0, 0)
#define BC(T, v) __builtin_bit_cast(T, v)
#define SC1 16

constexpr u32 WS_FF = 4096;
constexpr u32 WS_FI = WS_FF + 256 * 2560;
constexpr u32 WS_GR = WS_FI + 131072;
constexpr u32 WS_GS = WS_GR + 131072;
constexpr u32 WS_GB = WS_GS + 16384;
constexpr u32 WS_GW = WS_GB + 8192;
constexpr u32 WS_HF = 1310720;
constexpr u32 WS_HS = WS_HF + 16 * 4 * 56 * 2048;
constexpr u32 WS_X = WS_HS + 16 * 4 * 64 * 1024;
constexpr u32 WS_END = WS_X + 16 * 4 * 4 * 4 * 6144;
static_assert(WS_GW + 8 * 1792 * 8 <= WS_HF, "ws map");

constexpr int WB = 49152;
constexpr int RO[3] = {16384, 0, 32768};
constexpr int U1S[8] = {12, 13, 14, 15, 10, 11, 8, 9};
constexpr int W2S[14] = {16, 0, 1, 2, 3, 12, 13, 14, 15, 16, 0, 1, 2, 3};
constexpr int MI_TR = 139264, MI_PW = MI_TR, MI = MI_TR + 8 * 2304;
constexpr int MI_PS = MI, MI_CT = MI + 128, MI_RC = MI_CT + 1024, MI_RS = MI_RC + 2048, MI_NM = MI_RS + 512, MI_WT = MI_NM + 512, MI_HP = MI_WT + 1024,
              MI_CNT = MI_HP, MI_JUNK = MI_CNT + 128;
constexpr int MI_HQ = MI_CT;
constexpr int LDS_BYTES = 163840;
static_assert(MI_JUNK + 256 + 128 <= LDS_BYTES, "lds map");
constexpr int C_WT = 0, C_HP = 4, C_U1 = 8, C_LA = 12, C_LB = 16, C_LC = 20, C_LD = 24, C_P1 = 28, C_P2 = 32, C_P3 = 36, C_PAIR = 64;
constexpr int OOB = 0x40000000;
constexpr u32 SPIN_CAP = 1u << 18;
#define KD 22

#define STAMP(i)
#define SET 0
#define STS(set, i) do { if (SET == (set)) STAMP(i); } while (0)

DI float asf(u32 b) { return BC(float, b); }
DI u32 asu(float f) { return BC(u32, f); }
DI float bf2f(u32 b) { return asf(b << 16); }
DI float bfh(u32 b) { return asf(b & 0xffff0000u); }
DI u32 pk2(float a, float b) {
  f32x2 v = {a, b};
  return BC(u32, __builtin_convertvector(v, bf16x2n));
}
DI float rbf(float a) { return bf2f(pk2(a, a)); }
DI i32x4 mkrs(u64 a, u32 bytes) {
  i32x4 r = {(int)(u32)a, (int)((u32)(a >> 32) & 0xffffu), (int)bytes, 0x00020000};
  return r;
}
template <int SI, int LI> DI void dma1(i32x4 rs, int v, int sb, int lw) {
  int t;
  asm volatile("s_add_i32 m0, %[lw], %[li]\n\ts_add_i32 %[t], %[sb], %[si]\n\tbuffer_load_dwordx4 %[v], %[rs], %[t] offen lds"
               : [t] "=&s"(t)
               : [v] "v"(v), [rs] "s"(rs), [sb] "s"(sb), [lw] "s"(lw), [si] "n"(SI), [li] "n"(LI)
               : "memory", "scc");
}
template <int LI> DI void dma_s(i32x4 rs, int v, int& so, int ps, int lw) {
  asm volatile("s_add_i32 m0, %[lw], %[li]\n\ts_add_i32 %[t], %[t], %[ps]\n\tbuffer_load_dwordx4 %[v], %[rs], %[t] offen lds"
               : [t] "+s"(so)
               : [v] "v"(v), [rs] "s"(rs), [ps] "s"(ps), [lw] "s"(lw), [li] "n"(LI)
               : "memory", "scc");
}
DI void touch(const void* p, int lw) {
  asm volatile("s_mov_b32 m0, %1\n\ts_nop 0\n\tglobal_load_lds_dword %0, off" ::"v"(p), "s"(lw) : "memory");
}
template <int I> struct IC { static constexpr int v = I; };
template <int A, int B, class Fn> DI void sfor(Fn&& f) {
  if constexpr (A < B) {
    f(IC<A>{});
    sfor<A + 1, B>(f);
  }
}
typedef f32x2 cf;
template <class T> DI void pin1(T& v) { asm volatile("" : "+v"(v)); }
template <class... T> DI void pin(T&... v) { (pin1(v), ...); }
DI int opq(int v) { asm volatile("" : "+v"(v)); return v; }
DI int opqs(int v) { asm volatile("" : "+s"(v)); return v; }
template <int N> DI void bar() { asm volatile("s_waitcnt vmcnt(%0) lgkmcnt(0)\n\ts_barrier" ::"n"(N) : "memory"); }
template <int N> DI void waitv() { asm volatile("s_waitcnt vmcnt(%0)" ::"n"(N) : "memory"); }
DI void waitl() { asm volatile("s_waitcnt lgkmcnt(0)" ::: "memory"); }
DI bf16x8 ld128(int a) { return *(LAS bf16x8*)(size_t)a; }
DI bf16x8 ldtr(int a, int d) {
  bf16x4 lo = __builtin_amdgcn_ds_read_tr16_b64_v4i16((LAS bf16x4*)(size_t)a);
  bf16x4 hi = __builtin_amdgcn_ds_read_tr16_b64_v4i16((LAS bf16x4*)(size_t)(a + d));
  return __builtin_shufflevector(lo, hi, 0, 1, 2, 3, 4, 5, 6, 7);
}
#define LDS(T, a) (*(LAS T*)(size_t)(a))
DI float add32(float a, float b) {
  auto r = __builtin_amdgcn_permlane32_swap(asu(a), asu(b), false, false);
  return asf(r[0]) + asf(r[1]);
}
DI float add16(float a, float b) {
  auto r = __builtin_amdgcn_permlane16_swap(asu(a), asu(b), false, false);
  return asf(r[0]) + asf(r[1]);
}
template <int C> DI float dppadd(float v) { return v + asf((u32)__builtin_amdgcn_update_dpp(0, (int)asu(v), C, 0xf, 0xf, true)); }
DI float rsum(float v) {
  v = dppadd<0xB1>(v);
  v = dppadd<0x4E>(v);
  v = dppadd<0x141>(v);
  return dppadd<0x140>(v);
}
DI void halves(float v, float& a, float& b) {
  auto r = __builtin_amdgcn_permlane32_swap(asu(v), asu(v), false, false);
  a = asf(r[0]);
  b = asf(r[1]);
}
DI u32 split2(float c) {
  return pk2(c, c - rbf(c));
}
DI float dot2(u32 a, u32 b, float c) { return __builtin_amdgcn_fdot2_f32_bf16(BC(bf16x2n, a), BC(bf16x2n, b), c, false); }
DI cf pfma(cf a, cf b, cf c) {
  asm("v_pk_fma_f32 %0, %1, %2, %0 op_sel_hi:[0,1,1]\n\tv_pk_fma_f32 %0, %1, %2, %0 op_sel:[1,1,0] op_sel_hi:[1,0,1] neg_lo:[1,0,0]"
      : "+&v"(c)
      : "v"(a), "v"(b));
  return c;
}
DI cf pmul(cf a, cf b) {
  cf t;
  asm("v_pk_mul_f32 %0, %1, %2 op_sel_hi:[0,1]\n\tv_pk_fma_f32 %0, %1, %2, %0 op_sel:[1,1,0] op_sel_hi:[1,0,1] neg_lo:[1,0,0]"
      : "=&v"(t)
      : "v"(a), "v"(b));
  return t;
}
DI cf csq(cf a) { return pmul(a, a); }
DI float sfma(float a, float b, float c) { asm("v_fma_f32 %0, %1, %2, %3" : "=v"(c) : "v"(a), "v"(b), "v"(c)); return c; }
DI cf cfma(cf a, cf b, cf c) {
  float x = c.x, y = c.y;
  asm("v_fmac_f32 %0, %2, %4\n\tv_fmac_f32 %1, %2, %5\n\tv_fma_f32 %0, -%3, %5, %0\n\tv_fmac_f32 %1, %3, %4"
      : "+&v"(x), "+&v"(y)
      : "v"(a.x), "v"(a.y), "v"(b.x), "v"(b.y));
  return cf{x, y};
}
DI cf cmul(cf a, cf b) {
  float x, y;
  asm("v_mul_f32 %0, %2, %4\n\tv_mul_f32 %1, %2, %5\n\tv_fma_f32 %0, -%3, %5, %0\n\tv_fmac_f32 %1, %3, %4"
      : "=&v"(x), "=&v"(y)
      : "v"(a.x), "v"(a.y), "v"(b.x), "v"(b.y));
  return cf{x, y};
}
DI cf csel(bool c, cf a, cf b) { return cf{c ? a.x : b.x, c ? a.y : b.y}; }
DI void settle(cf& a) { asm volatile("s_nop 1" : "+v"(a)); }
template <int O> DI void sig(int sv) { asm volatile("ds_inc_u32 %0, %0 offset:%1" ::"v"(sv), "n"(O) : "memory"); }
template <int O> DI u32 peek(int sv) { return *(volatile LAS u32*)(size_t)(sv + O); }
template <int O, int N> DI void chk(int sv, u32 v) {
  int t, c;
  asm volatile("v_readfirstlane_b32 %[t], %[v]\n\ts_cmp_ge_u32 %[t], %[n]\n\ts_cbranch_scc1 2f\n\ts_mov_b32 %[c], 0x400000\n"
               "1:\n\ts_sleep 1\n\tds_read_b32 %[v], %[a] offset:%[o]\n\ts_sub_u32 %[c], %[c], 1\n\ts_waitcnt lgkmcnt(0)\n\ts_cbranch_scc1 2f\n\t"
               "v_readfirstlane_b32 %[t], %[v]\n\ts_cmp_lt_u32 %[t], %[n]\n\ts_cbranch_scc1 1b\n2:"
               : [t] "=&s"(t), [c] "=&s"(c), [v] "+v"(v)
               : [a] "v"(sv), [o] "n"(O), [n] "n"(N)
               : "memory", "scc");
}
template <int O, int N> DI void lwait(int sv) { chk<O, N>(sv, peek<O>(sv)); }
DI void ldg4(u32x4& d, int vo, i32x4 rs, int so) {
  asm volatile("buffer_load_dwordx4 %0, %1, %2, %3 offen sc1" : "=v"(d) : "v"(vo), "s"(rs), "s"(so) : "memory");
}
template <int O> DI void ldg4o(u32x4& d, int vo, i32x4 rs, int so) {
  asm volatile("buffer_load_dwordx4 %0, %1, %2, %3 offen offset:%4 sc1" : "=v"(d) : "v"(vo), "s"(rs), "s"(so), "n"(O) : "memory");
}
DI void ldg2(u32x2& d, int vo, i32x4 rs) { asm volatile("buffer_load_dwordx2 %0, %1, %2, 0 offen sc1" : "=v"(d) : "v"(vo), "s"(rs) : "memory"); }
template <int O> DI void stg4(u32x4 d, int vo, i32x4 rs, int so) {
  asm volatile("buffer_store_dwordx4 %0, %1, %2, %3 offen offset:%4 sc1" ::"v"(d), "v"(vo), "s"(rs), "s"(so), "n"(O) : "memory");
}
template <int O> DI void stg2(u32x2 d, int vo, i32x4 rs) {
  asm volatile("buffer_store_dwordx2 %0, %1, %2, 0 offen offset:%3 sc1" ::"v"(d), "v"(vo), "s"(rs), "n"(O) : "memory");
}

constexpr int aextra(int n) { return n == 17 ? 4 : ((n == 1 || n == 21 || n == 23 || (n >= 6 && n <= 16 && !(n & 1))) ? 1 : 0); }
constexpr int abar(int nb) {
  int c = (nb >= 6 && nb != 22) ? aextra(nb - 5) : 0;
  for (int m = nb < 6 ? 0 : nb - 4; m < nb; ++m) c += (m < 2 ? 2 : 1) + aextra(m);
  return c;
}
#define KS 6
#define KT 4
#define GA0 0
#define GA1 56
#define GB0 66
#define GB1 101
#define DF 16
#define LD0 24
#define LD1 24
#define LD2 26
#define LD3 26
#define LD4 18
constexpr int gapof(int k, int i) { return 64 + (k < KD ? 4 * k + i : 4 * KD + 3 * (k - KD) + i - 1); }
constexpr int gap1(int k) { return gapof(k, k < KD ? 0 : 1); }
constexpr int gsub(int g, int g0, int g1) { return g <= g0 ? 0 : (g >= g1 ? 66 : ((g - g0) * 66 + (g1 - g0) - 1) / (g1 - g0)); }
constexpr int gdone(int t, int g0, int g1) { int g = g0; while (gsub(g + 1, g0, g1) <= t) ++g; return g; }
enum { E_ST, E_SS, E_FL, E_LD };
struct Ev { int gap, kind, arg; };
struct EvTab { Ev e[64]; int n; };
constexpr int GK[6] = {10, 16, 22, 34, 40, 42};
constexpr int LDA[5] = {LD0, LD1, LD2, LD3, LD4};
constexpr int chkgap(int g) { return gap1(GK[g]) - LDA[g]; }
constexpr EvTab mkev() {
  EvTab t{};
  int sg[8] = {};
  for (int f = 0; f < 8; ++f) {
    sg[f] = f < 4 ? gdone(16 * f + 17, GA0, GA1) : gdone(16 * (f - 4) + 17, GB0, GB1);
    t.e[t.n++] = Ev{sg[f], E_ST, f};
  }
  for (int f = 0; f < 4; ++f) t.e[t.n++] = Ev{gap1(KS) + 2 * f, E_SS, f};
  t.e[t.n++] = Ev{sg[1] + DF, E_FL, 0};
  t.e[t.n++] = Ev{sg[3] + DF, E_FL, 1};
  t.e[t.n++] = Ev{gap1(KS) + 6 + DF, E_FL, 2};
  t.e[t.n++] = Ev{sg[5] + DF, E_FL, 3};
  t.e[t.n++] = Ev{sg[7] + DF, E_FL, 4};
  for (int g = 0; g < 5; ++g)
    for (int k = GK[g]; k < GK[g + 1]; ++k) t.e[t.n++] = Ev{chkgap(g) + 2 * (k - GK[g]), E_LD, k};
  for (int a = 1; a < t.n; ++a)
    for (int c = a; c > 0 && t.e[c - 1].gap > t.e[c].gap; --c) {
      const Ev x = t.e[c];
      t.e[c] = t.e[c - 1];
      t.e[c - 1] = x;
    }
  return t;
}
constexpr EvTab EV = mkev();
constexpr int evfirst(int g) { int a = 0; while (a < EV.n && EV.e[a].gap < g) ++a; return a; }
constexpr int evfind(int kind, int arg) { int a = 0; while (EV.e[a].kind != kind || EV.e[a].arg != arg) ++a; return a; }
constexpr int flaglast(int g) { return g == 2 ? evfind(E_SS, 3) : evfind(E_ST, g < 2 ? 2 * g + 1 : 2 * g - 1); }
static_assert(gdone(33, GA0, GA1) < gap1(1) && gdone(65, GA0, GA1) < gap1(3) && GA1 <= GB0, "first unit");
static_assert(gdone(17, GB0, GB1) < gap1(KT) && gdone(33, GB0, GB1) < gap1(KT + 1) && GB0 >= 65, "second unit");
static_assert(KD >= 14 && KD <= 32, "late steps");

typedef u32 u32x16 __attribute__((ext_vector_type(16)));
typedef u32 u32x8 __attribute__((ext_vector_type(8)));
#define GAS __attribute__((address_space(1)))
template <class V> DI u64 kp(V v, int i) { return (u64)v[2 * i] | ((u64)v[2 * i + 1] << 32); }

template <bool ISA> DI void role(const int w, int tid, const u64 ka, const u32 j0, const u32 j1, const u64 tstart) {
  u32x16 k0;
  u32x8 k1;
  u32x4 k2;
  u32x2 k3;
  asm volatile("s_load_dwordx16 %0, %1, 0" : "=s"(k0) : "s"(ka) : "memory");
  if constexpr (ISA)
    asm volatile("s_load_dwordx8 %0, %3, 64\n\ts_load_dwordx4 %1, %3, 96\n\ts_load_dwordx2 %2, %3, 112" : "=&s"(k1), "=&s"(k2), "=&s"(k3) : "s"(ka) : "memory");
  if constexpr (ISA) asm volatile("; A" : "+v"(tid));
  else asm volatile("; B" : "+v"(tid));
  const int lane = tid & 63;
  const int b = blockIdx.x, xcd = b & 7, q8 = b >> 3;
  const int g = xcd & 3, r = (xcd >> 2) * 8 + (q8 >> 2), j = q8 & 3, s = 4 * g + j;
  const int u0 = 7 * g + ((0x5320 >> (4 * j)) & 15), u1 = 7 * g + ((0x6411 >> (4 * j)) & 15);
  const int rg = w & 3, h = lane >> 5, l31 = lane & 31;
  const int rl = lane >> 3, pos = lane & 7, gi = lane >> 4;
  const int blk = r * 4 + j;
  const int l16 = lane * 16;

  int ox = (r * 128 + s * 8 + w) * 1024 + l16;
  int ow = ((blk & 7) * 64 + w * 8 + rl) * 3584 + (7 * g + (blk < 56 ? blk >> 3 : 0)) * 128 + pos * 16;
  int ob = (s * 32 + r * 2 + (rg >> 1)) * 1024 + l16;
  const int ctc = ((rg >> 1) ? u1 : u0) * 64 + ((rg & 1) ^ (rg >> 1 & (j == 1))) * 32;
  int ol = (s * 32 + l31) * 2, od = (ctc + pos * 4 + gi) * 2;
  const int vxE = rl * 1024 + ((pos ^ (rl >> 1)) << 4), vxO = vxE ^ 64;
  const int vw1 = rl * 3584 + ((pos ^ ((rl & 2) << 1)) << 4);
  const bool xt = ISA || w < 6;
  int vmE = xt ? vxE : vw1, vmO = xt ? vxO : vw1;
  const int pstr = xt ? 8192 : 8 * 3584, cadv = xt ? 128 - 3 * 8192 : 40 * 3584;
  const int mb1 = ISA ? 0 : opqs(s * 32768), mb2 = ISA ? 0 : opqs((w - 6) * 32 * 3584 + u0 * 128);
  int mso = opqs((ISA ? (r * 128 + w * 32) * 1024 : (xt ? mb1 : mb2)) - cadv);
  const int lw4 = opqs(rg * 4096 + (ISA ? 0 : WB));
  const u32 msz = ISA ? 2048u * 1024u : opqs(xt ? 512u * 1024u : 512u * 3584u);
  u32x4 ax_, aw_, ab_ = {};
  u32 pa, pb, pc, pd;
  pin(ox, ow, vmE, vmO);
  if constexpr (ISA) pin(ob, ol, od);
  SB;
  if constexpr (ISA) asm volatile("s_waitcnt lgkmcnt(0)" : "+s"(k0), "+s"(k1), "+s"(k2), "+s"(k3) : "s"(j0), "s"(j1) : "memory");
  else asm volatile("s_waitcnt lgkmcnt(0)" : "+s"(k0) : "s"(j0), "s"(j1) : "memory");
  const u64 x = kp(k0, 0), w1 = kp(k0, 1), bre = kp(k0, 2), bim = kp(k0, 3), ws = kp(k0, 4), b1 = kp(k0, 5), lre = kp(k0, 6), lim = kp(k0, 7);
  const u64 ldt = kp(k1, 0), cre = kp(k1, 1), cim = kp(k1, 2), w2 = kp(k1, 3), dsk = kp(k2, 0), b2 = kp(k2, 1), out = kp(k3, 0);
  if constexpr (ISA) {
    asm volatile("global_load_ushort %0, %1, %2" : "=v"(pa) : "v"(ol), "s"(lre) : "memory");
    asm volatile("global_load_ushort %0, %1, %2" : "=v"(pb) : "v"(ol), "s"(lim) : "memory");
    asm volatile("global_load_ushort %0, %1, %2" : "=v"(pc) : "v"(ol), "s"(ldt) : "memory");
    asm volatile("global_load_ushort %0, %1, %2" : "=v"(pd) : "v"(od), "s"(b1) : "memory");
    asm volatile("global_load_dwordx4 %0, %1, %2" : "=v"(ab_) : "v"(ob), "s"((w & 1) ? bim : bre) : "memory");
  }
  asm volatile("global_load_dwordx4 %0, %1, %2" : "=v"(ax_) : "v"(ox), "s"(x) : "memory");
  asm volatile("global_load_dwordx4 %0, %1, %2" : "=v"(aw_) : "v"(ow), "s"(w1) : "memory");
  const i32x4 rm = mkrs(ISA ? x : (w == 4 ? bre : (w == 5 ? bim : w1)), msz);
#define PIECE(c, p) dma_s<RO[(c) % 3] + (p) * 1024>(rm, ((p) & 1) ? vmO : vmE, mso, (p) == 0 ? cadv : pstr, lw4)
  PIECE(0, 0); PIECE(0, 1); PIECE(0, 2); PIECE(0, 3);
  u64 tag;
  asm volatile("s_load_dwordx2 %0, %1, 0" : "=s"(tag) : "s"(ws) : "memory");
  const i32x4 rw = mkrs(ws, WS_END);
  const int sv = opq(lane == 0 ? MI_CNT : MI_JUNK + lane * 4);
  if constexpr (!ISA) LDS(u32, MI_CNT + l31 * 4) = 0u;
  const int ax = opq(l31 * 128 + (((h ^ (l31 >> 1)) & 7) << 4));
  const int tq = (lane >> 2) & 3, tp = lane & 3;
  int xa[4];
#pragma unroll
  for (int k = 0; k < 4; ++k) xa[k] = opq((ax ^ (k * 32)) + rg * 4096);
  const u32 ordl = opqs((j & 2) ? ((j & 1) ? 0x51640CBAu : 0x51A40876u) : ((j & 1) ? 0x71A60354u : 0x75A64210u));
  const u32 ordh = opqs((j & 2) ? ((j & 1) ? 0xD98327u : 0x9DC32Bu) : ((j & 1) ? 0xD9C82Bu : 0xD9C83Bu));
  auto hold = [&](int at) AI {
    if (at == HOLDAT) {
      u64 t;
      do { asm volatile("s_sleep 2\n\ts_memrealtime %0\n\ts_waitcnt lgkmcnt(0)" : "=s"(t)::"memory"); } while (t < tstart + HOLD);
    }
  };
  auto done = [&]() AI {
    hold(15);
  };
  u32 tlo, thi;
  auto stats = [&]() AI {
    asm volatile("s_waitcnt vmcnt(4) lgkmcnt(0)" : "+v"(ax_), "+v"(aw_), "+s"(tag)::"memory");
    if constexpr (ISA) pin(ab_);
    tag += 1ull;
    tlo = opqs((u32)tag);
    thi = opqs((u32)(tag >> 32));
    STAMP(1);
    PIECE(1, 0);
    {
      float sm = 0.f, sq = 0.f, bs = 0.f, y[8];
#pragma unroll
      for (int i = 0; i < 4; ++i) {
        sm = dot2(ax_[i], 0x3f803f80u, sm);
        sq = dot2(ax_[i], ax_[i], sq);
        if constexpr (ISA) bs = dot2(ab_[i], 0x3f803f80u, bs);
      }
      sm = add32(sm, sq);
      sm = add16(sm, ISA ? add32(bs, bs) : sm);
      pin(sm);
      SB;
      PIECE(1, 1);
#pragma unroll
      for (int i = 0; i < 4; ++i) {
        y[2 * i] = bf2f(aw_[i]);
        y[2 * i + 1] = bfh(aw_[i]);
      }
      LDS(float, MI_PS + w * 16 + gi * 4) = rsum(sm);
#pragma unroll
      for (int i = 0; i < 4; ++i) y[i] = add32(y[i], y[i + 4]);
      pin(y[0], y[1], y[2], y[3]);
      SB;
      PIECE(1, 2);
      const float z0 = dppadd<0x128>(add16(y[0], y[2])), z1 = dppadd<0x128>(add16(y[1], y[3]));
      LDS(f32x2, MI_PW + w * 256 + (pos * 8 + gi * 2) * 4) = f32x2{z0, z1};
      SB;
      PIECE(1, 3);
    }
    STAMP(2);
  };

  const i32x4 r1 = mkrs(w1, 512u * 3584u);
  const int w1b = w * 8 * 3584 + u1 * 128, lw1 = w * 1024;
  auto w1p = [&](auto Cc) AI {
    constexpr int c = decltype(Cc)::v;
    dma1<c * 64 * 3584, U1S[c] * 8192>(r1, vw1, w1b, lw1);
  };
  auto tile = [&](int p) AI { return (int)((opqs(p >= 8 ? ordh : ordl) >> (4 * (p & 7))) & 15); };
  const int trb = MI_TR + rg * 4608;
  const int svp = sv + rg * 16;
  const int ffb = (int)WS_FF + ((r * 4 + g) * 4 + rg) * 2560;

  if constexpr (ISA) {
    cf l1, l2, l3, l4, l32, l256, co2, lF, lL, mq[4];
    {
      asm volatile("s_waitcnt vmcnt(7)" : "+v"(pa), "+v"(pb), "+v"(pc), "+v"(pd)::"memory");
      const float lr = bf2f(pa), li = bf2f(pb), dt = rbf(expf(bf2f(pc))), er = expf(dt * lr), th = dt * li;
      const float n = __builtin_rintf(th * 0.636619772f);
      const float y = fmaf(n, 1.71512451e-15f, fmaf(n, 4.37113883e-8f, fmaf(n, -1.57079637f, th))), z = y * y;
      const float pc_ = fmaf(z * z, fmaf(z, fmaf(z, 2.44331571e-5f, -1.38873163e-3f), 4.16666457e-2f), fmaf(z, -0.5f, 1.0f));
      const float ps_ = fmaf(z * y, fmaf(z, fmaf(z, -1.95152959e-4f, 8.33216087e-3f), -1.66666546e-1f), y);
      const u32 qd = (u32)(int)n;
      float sn = asf(asu((qd & 1) ? pc_ : ps_) ^ ((qd & 2) << 30)), cs = asf(asu((qd & 1) ? ps_ : pc_) ^ (((qd + 1) & 2) << 30));
      if (__builtin_expect(__builtin_amdgcn_ballot_w64(!(fabsf(th) < 8192.f)) != 0, 0)) sincosf(th, &sn, &cs);
      l1 = cf{er * cs, er * sn};
      const float den = 2.0f / fmaf(lr, lr, li * li);
      co2 = pmul(cf{l1.x - 1.0f, l1.y}, cf{lr * den, -li * den});
      const cf one = {1.f, 0.f};
      l2 = csq(l1);
      l3 = pmul(l2, l1);
      l4 = csq(l2);
      const cf l8 = csq(l4);
      l32 = csq(csq(l8));
      const cf l64 = csq(l32), l128 = csq(l64);
      l256 = csq(l128);
      const cf l512 = csq(l256);
      lF = pmul(pmul(csel(rg & 1, l512, one), csel(rg & 2, csq(l512), one)), csel(h, l128, one));
      lL = pmul(csel(rg & 1, l32, one), csel(rg & 2, l64, one));
      mq[0] = pmul(co2, csel(h, l4, one));
      mq[1] = pmul(mq[0], l8);
      mq[2] = pmul(mq[1], l8);
      mq[3] = pmul(mq[2], l8);
      pin(l1, l2, l3, l4, l32, l256, co2, lF, lL, mq[0], mq[1], mq[2], mq[3]);
    }
    stats();
    f32x16 are = {}, aim = {};
    bf16x8 fxr[3], fbr[3], fbi[3];
    int xb[4];
#pragma unroll
    for (int k = 0; k < 4; ++k) xb[k] = opq((ax ^ (k * 32)) + WB);
    auto rdi = [&](int n) AI { fbi[n % 3] = ld128(xb[n & 3] + RO[(n >> 2) % 3] + 4096); };
    auto rdx = [&](int n) AI {
      fbr[n % 3] = ld128(xb[n & 3] + RO[(n >> 2) % 3]);
      fxr[n % 3] = ld128(xa[n & 3] + RO[(n >> 2) % 3]);
    };
    const i32x4 rcr = mkrs(cre, 512u * 1024u), rci = mkrs(cim, 512u * 1024u), r2 = mkrs(w2, 1792u * 1024u);
    const int lw2 = rg * 2048, w2b = (448 * g + rg * 8) * 1024 + j * 256;
    int vw2;
    auto w2t = [&](auto Pp) AI {
      constexpr int p = decltype(Pp)::v;
      const int sb = w2b + (tile(p) << 15);
      dma1<0, W2S[p] * 8192>(r2, vw2, sb, lw2);
      dma1<4096, W2S[p] * 8192 + 1024>(r2, vw2, sb, lw2);
    };
    const int cb0 = opqs((j * 128 + (((rg >> 1) ^ g) * 32) + (rg & 1) * 16) * 1024 + g * 256 + (j >> 1) * 128);
    auto ctile = [&](auto Pp) AI {
      constexpr int p = decltype(Pp)::v;
      const int sb = opqs(cb0) ^ ((p & 1) * 65536 + (p >> 2) * 128);
      dma1<0, (4 + p) * 8192>((p & 2) ? rci : rcr, vxE, sb, lw2);
      dma1<8192, (4 + p) * 8192 + 1024>((p & 2) ? rci : rcr, vxO, sb, lw2);
    };
    float t0, t1;
    int o1, o2, o3, a8, aw, at, lo0, lo1, fo;
    u32x2 gs;
    u32x4 gw0, gw1, gb;
    u32 nm;
    f32x4 rsv[4], nmv[4];
    const char* tch;
    const u64 csrc = (u64)((rg & 1) ? cim : cre);
    const int d0 = r - 1 - 4 * rg - h;
    const int gro = opqs((int)WS_GR + (s * 16 + r) * 512), grs = opqs((int)WS_GR + s * 8192);
    int cvo;
    auto gather = [&]() AI {
      asm volatile("buffer_load_dwordx2 %0, %4, %7, 0 offen sc1\n\tbuffer_load_dwordx4 %1, %5, %7, 0 offen sc1\n\t"
                   "buffer_load_dwordx4 %2, %5, %7, 0 offen offset:16 sc1\n\tbuffer_load_dwordx4 %3, %6, %7, 0 offen sc1"
                   : "=&v"(gs), "=&v"(gw0), "=&v"(gw1), "=&v"(gb)
                   : "v"(o1), "v"(o2), "v"(o3), "s"(rw)
                   : "memory");
    };
    const int xin = opqs((int)WS_X + (((r * 4 + j) * 4 + g) * 4 + rg) * 6144);
    u32x4 none;
    auto side = [&](auto Nn) AI {
      constexpr int n = decltype(Nn)::v;
      if constexpr (n == 3) {
        none = u32x4{~0u, ~0u, ~0u, ~0u};
        pin(none);
      }
      if constexpr (n >= 6 && n <= 16 && !(n & 1)) stg4<((n - 6) * 512) & 4095>(none, l16, rw, xin + (((n - 6) * 512) & 4096));
      if constexpr (n == 23) stg2<0>(u32x2{tlo, thi}, lane == 0 ? (int)WS_FI + (((r * 4 + j) * 4 + rg) * 4 + g) * 128 : OOB, rw);
      if constexpr (n == 1) {
        u32 val = 0;
        int o = OOB;
        if (w == 2) {
          const int a = opq(MI_PW + lane * 4);
          float c[8];
#pragma unroll
          for (int i = 0; i < 8; ++i) c[i] = LDS(float, a + i * 256);
          o = (blk < 56 ? (int)WS_GW + ((blk & 7) * 1792 + (7 * g + (blk >> 3)) * 64) * 8 : OOB) + lane * 8;
          val = asu(((c[0] + c[1]) + (c[2] + c[3])) + ((c[4] + c[5]) + (c[6] + c[7])));
        } else if (w == 3) {
          const f32x4 p = LDS(f32x4, MI_PS + pos * 16);
          const int oa = opqs((int)WS_GS + (r * 128 + s * 8) * 8), oc = opqs((int)WS_GB + (s * 32 + r * 2) * 16 - 64);
          o = (lane < 8 ? oa : (lane < 12 ? oc : OOB)) + lane * 8;
          const float m = p.x * (1.0f / 512.0f), v = fmaxf(fmaf(-m, m, p.z * (1.0f / 512.0f)), 0.f);
          val = lane < 8 ? pk2(m, __builtin_amdgcn_sqrtf(rbf(rbf(v) + 1e-5f))) : split2(p.y);
        }
        stg2<0>(u32x2{val, tlo}, o, rw);
      } else if constexpr (n == 4) {
        o1 = opq((int)WS_GS + (r * 128 + rg * 32 + l31) * 8);
        o2 = opq((int)WS_GW + (rl * 1792 + ctc + pos * 4) * 8);
        o3 = opq((int)WS_GB + (s * 32 + l31) * 16);
      } else if constexpr (n == 5) {
        vw2 = opq(gi * 1024 + (((lane & 15) ^ ((gi ^ g) << 2)) << 4));
        a8 = opq(MI_CT + l31 * 8);
        cvo = opq(lane < (w == 3 ? 32 : 0) ? l16 : OOB);
      } else if constexpr (n == 7) {
        aw = opq(MI_CT + rg * 256 + l31 * 8 + h * 4);
        at = opq(trb + l31 * 72 + h * 8);
      } else if constexpr (n == 8) {
        lo0 = opq(d0 >= 0 ? d0 * 512 + l31 * 16 : OOB);
        lo1 = opq(d0 >= 2 ? (d0 - 2) * 512 + l31 * 16 : OOB);
      } else if constexpr (n == 9) {
        const bool ln = lane < 14;
        const int oa = (g * 3584 + blk * 56 + rg * 14 + lane) * 128, oc = (blk * 8 + (rg >> 1) * 4 + (pos >> 1)) * 1024 + g * 256 + (pos & 1) * 128;
        const int o = ln ? oa : oc;
        const u64 p2 = (u64)w2, p3 = csrc;
        tch = (const char*)(ln ? p2 : p3) + o;
        pin(tch);
      } else if constexpr (n == 11) {
        const u32 nd = (0xCFFFFu & ~(0x11111u << j)) | (0xF00000u & ~(0x100000u << g));
        const int fi = opqs((int)WS_FI + ((r * 4 + j) * 4 + rg) * 512 - 2560), ff = opqs(ffb);
        fo = opq((((nd >> l31) & 1) > h) ? (lane < 20 ? ff : fi) + lane * 128 : OOB);
      } else if constexpr (n == 17) {
        gather();
      } else if constexpr (n == 21) {
        touch(tch, MI_JUNK);
      } else if constexpr (n == 22) {
        pin(gs, gw0, gw1, gb);
        _Pragma("nounroll") for (u32 it = 0; it < SPIN_CAP; ++it) {
          const u32 d = ((gs.y ^ tlo) | (gw0.y ^ tlo)) | ((gw0.w ^ tlo) | (gw1.y ^ tlo)) | ((gw1.w ^ tlo) | (gb.y ^ tlo)) | (gb.w ^ tlo);
          if (__builtin_expect(__builtin_amdgcn_ballot_w64(d != 0) == 0, 1)) break;
          gather();
          asm volatile("s_waitcnt vmcnt(0)" : "+v"(gs), "+v"(gw0), "+v"(gw1), "+v"(gb)::"memory");
        }
      } else if constexpr (n == 23) {
        t0 = dppadd<0x128>(add16(add32(asf(gw0.x), asf(gw1.x)), add32(asf(gw0.z), asf(gw1.z))));
        pin(t0);
      } else if constexpr (n == 24) {
        const u32 p = split2(t0);
        LDS(u32x2, MI_CT + (rg * 32 + pos * 4 + gi) * 8) = u32x2{pd | (p << 16), p >> 16};
      } else if constexpr (n == 25) {
        t0 = bfh(gs.x);
        t1 = __builtin_amdgcn_rcpf(t0);
        nm = (gs.x & 0xffffu) ^ 0x8000u;
        t1 = fmaf(fmaf(-t0, t1, 1.0f), t1, t1);
        pin(t1, nm);
      } else if constexpr (n == 26) {
        const float gc0 = -2.302208198f * t1;
        LDS(u32x4, MI_RC + (rg * 32 + l31) * 16) = u32x4{asu(gc0), asu(gc0 * 0.044715f * (t1 * t1)), (gs.x >> 16) | (nm << 16), nm};
      } else if constexpr (n == 27) {
        LDS(float, MI_RS + h * 512 + (rg * 32 + l31) * 4) = h ? bf2f(nm) * t1 : t1;
        nm = h ? 0u : nm | (nm << 16);
        pin(nm);
      } else if constexpr (n == 28) {
        are = MFMA32(BC(bf16x8, (u32x4{nm, 0u, 0u, 0u})), BC(bf16x8, (u32x4{gb.x, 0u, 0u, 0u})), are); pin(are);
        aim = MFMA32(BC(bf16x8, (u32x4{nm, 0u, 0u, 0u})), BC(bf16x8, (u32x4{gb.z, 0u, 0u, 0u})), aim); pin(aim);
      } else if constexpr (n == 29) {
        const int a = opq(MI_RS + (rg * 32 + h * 4) * 4);
#pragma unroll
        for (int q = 0; q < 4; ++q) {
          rsv[q] = LDS(f32x4, a + q * 32);
          nmv[q] = LDS(f32x4, a + 512 + q * 32);
        }
      }
    };
    SB;
    bar<4>();
    STAMP(3);
    hold(3);
    rdi(0); rdx(0); rdi(1); rdx(1);
    sfor<0, 32>([&](auto Nn) AI {
      constexpr int n = decltype(Nn)::v, c = n >> 2, ks = n & 3;
      SB;
      are = MFMA32(fxr[n % 3], fbr[n % 3], are); pin(are);
      SB;
      if (ks != 2 && n + 2 < 32) rdi(n + 2);
      if constexpr (n < 2) PIECE(2, 2 * n);
      if constexpr (ks != 2) {
        if constexpr (n < 2) PIECE(2, 2 * n + 1);
        else if constexpr (n < 22) PIECE(((n - 2) >> 2) + 3, (n - 2) & 3);
        else if constexpr (n < 30) w1p(IC<n - 22>{});
        else w2t(IC<1>{});
      }
      SB;
      aim = MFMA32(fxr[n % 3], fbi[n % 3], aim); pin(aim);
      SB;
      if constexpr (ks == 2) {
        bar<abar(n)>();
        if (n + 2 < 32) rdi(n + 2);
      }
      if (n + 2 < 32) rdx(n + 2);
      if constexpr (ks == 2) {
        if constexpr (n < 22) PIECE(((n - 2) >> 2) + 3, 0);
        else if constexpr (n < 30) w1p(IC<n - 22>{});
        else w2t(IC<0>{});
      }
      SB;
      side(Nn);
    });
    SB;
    STAMP(4);
    hold(4);
    PRIO(2);
    pin(rsv[0], rsv[1], rsv[2], rsv[3]);
    cf u[16], sq[4];
#pragma unroll
    for (int i = 0; i < 16; ++i) {
      float a = are[i] * rsv[i >> 2][i & 3], c = aim[i] * rsv[i >> 2][i & 3];
      pin(a, c);
      u[i] = cf{a, c};
    }
    {
#pragma unroll
      for (int k = 1; k < 4; ++k)
#pragma unroll
        for (int q = 0; q < 4; ++q) u[4 * q + k] = cfma(l1, u[4 * q + k - 1], u[4 * q + k]);
      cf S = {0.f, 0.f};
#pragma unroll
      for (int q = 0; q < 4; ++q) {
        float ar, ai, cr, ci;
        halves(u[4 * q + 3].x, ar, cr);
        halves(u[4 * q + 3].y, ai, ci);
        const cf Sa = S;
        S = q ? cfma(l4, S, cf{ar, ai}) : cf{ar, ai};
        sq[q] = csel(h, S, Sa);
        S = cfma(l4, S, cf{cr, ci});
      }
      LDS(f32x2, a8 + (MI_WT - MI_CT) + rg * 256) = S;
    }
    cf t4[4];
    auto meet = [&](auto Oo, int a) AI {
      constexpr int o = decltype(Oo)::v;
      sig<o>(sv);
      _Pragma("nounroll") for (u32 it = 0; it < (SPIN_CAP << 4); ++it) {
        const u32 c = peek<o>(sv);
#pragma unroll
        for (int i = 0; i < 4; ++i) t4[i] = *(volatile LAS f32x2*)(size_t)(a + i * 256);
        if ((u32)__builtin_amdgcn_readfirstlane(c) >= 4u) break;
      }
    };
    meet(IC<C_WT>{}, a8 + (MI_WT - MI_CT));
    cf lin;
    {
      const cf s1 = cfma(l32, t4[0], t4[1]), s2 = cfma(l32, s1, t4[2]), s3 = cfma(l32, s2, t4[3]);
      stg4<0>(u32x4{asu(s3.x), tlo, asu(s3.y), tlo}, cvo, rw, gro);
      PRIO(0);
      lin = csel(rg & 2, csel(rg & 1, s2, s1), csel(rg & 1, t4[0], cf{0.f, 0.f}));
    }
    STAMP(5);
    waitv<5>();
    sig<C_U1>(sv);
    ctile(IC<0>{}); ctile(IC<1>{});
    PRIO(2);
    pin(co2);
#pragma unroll
    for (int i = 0; i < 16; ++i) u[i] = cmul(co2, u[i]);
#pragma unroll
    for (int q = 0; q < 4; ++q) sq[q] = cmul(co2, sq[q]);
#pragma unroll
    for (int q = 0; q < 4; ++q) pin(u[4 * q], u[4 * q + 1], u[4 * q + 2], u[4 * q + 3], sq[q]);
    pin(lin);
    PRIO(0);
    ctile(IC<2>{}); ctile(IC<3>{});
    w2t(IC<2>{}); w2t(IC<3>{}); w2t(IC<4>{});
    u32x4 lb0, lb1;
    ldg4(lb0, lo0, rw, grs);
    ldg4(lb1, lo1, rw, grs);
    lwait<C_P1, 4>(sv);
    w2t(IC<5>{}); w2t(IC<6>{}); w2t(IC<7>{}); w2t(IC<8>{});
    STAMP(6);
    asm volatile("s_waitcnt vmcnt(8)" : "+v"(lb0), "+v"(lb1)::"memory");
    sig<C_LA>(sv);
    PRIO(2);
    _Pragma("nounroll") for (u32 it = 0; it < SPIN_CAP; ++it) {
      const bool bad = ((d0 >= 0) & (((lb0.y ^ tlo) | (lb0.w ^ tlo)) != 0)) | ((d0 >= 2) & (((lb1.y ^ tlo) | (lb1.w ^ tlo)) != 0));
      if (__builtin_amdgcn_ballot_w64(bad) == 0) break;
      ldg4(lb0, lo0, rw, grs);
      ldg4(lb1, lo1, rw, grs);
      asm volatile("s_waitcnt vmcnt(0)" : "+v"(lb0), "+v"(lb1)::"memory");
    }
    {
      cf a = cmul(lF, cfma(l256, cf{asf(lb1.x), asf(lb1.z)}, cf{asf(lb0.x), asf(lb0.z)}));
      settle(a);
      LDS(float, aw) = add32(a.x, a.y);
    }
    meet(IC<C_HP>{}, a8);
    STAMP(7);
    {
      float zr = (t4[0].x + t4[1].x) + (t4[2].x + t4[3].x);
      pin(zr);
      const cf z = cfma(lL, cf{zr, (t4[0].y + t4[1].y) + (t4[2].y + t4[3].y)}, lin);
#pragma unroll
      for (int q = 0; q < 4; ++q) {
        const cf t = cfma(mq[q], z, sq[q]);
        cf v[4];
#pragma unroll
        for (int k = 0; k < 4; ++k) v[k] = cfma(k == 0 ? l1 : (k == 1 ? l2 : (k == 2 ? l3 : l4)), t, u[4 * q + k]);
        LDS(u32x2, at + 16 * q) = u32x2{pk2(v[0].x, v[1].x), pk2(v[2].x, v[3].x)};
        LDS(u32x2, at + 2304 + 16 * q) = u32x2{pk2(-v[0].y, -v[1].y), pk2(-v[2].y, -v[3].y)};
      }
    }
    sig<C_PAIR>(svp);
    PRIO(0);
    STAMP(8);
    u32x2 pv;
    int prog = 0;
    auto seen = [&]() AI {
      pin(pv);
      const bool bad = (fo != OOB) & (((pv.x ^ tlo) | (pv.y ^ thi)) != 0);
      fo = bad ? fo : OOB;
      prog = __builtin_ctzll(__builtin_amdgcn_ballot_w64(bad) | (1ull << 24)) >> 2;
      *(volatile LAS u32*)(size_t)(svp + C_PAIR + 4) = (u32)prog;
    };
    auto watch = [&](auto Oo) AI {
      constexpr int o = decltype(Oo)::v;
      _Pragma("nounroll") for (u32 it = 0; it < SPIN_CAP; ++it) {
        if constexpr (o >= 0) {
          if ((u32)__builtin_amdgcn_readfirstlane(peek<o>(sv)) >= 4u) break;
        } else {
          if (prog == 6) break;
        }
        ldg2(pv, fo, rw);
        waitv<0>();
        seen();
      }
    };
    watch(IC<C_P2>{});
    ldg2(pv, fo, rw);
    ctile(IC<4>{}); ctile(IC<5>{}); ctile(IC<6>{}); ctile(IC<7>{});
    waitv<8>();
    sig<C_LB>(sv);
    seen();
    ldg2(pv, fo, rw);
    waitv<0>();
    sig<C_LC>(sv);
    seen();
    STAMP(9);
    watch(IC<C_P3>{});
    STAMP(10);
    ldg2(pv, fo, rw);
    w2t(IC<9>{}); w2t(IC<10>{}); w2t(IC<11>{}); w2t(IC<12>{}); w2t(IC<13>{});
    waitv<10>();
    seen();
    ldg2(pv, fo, rw);
    float ep[16];
    {
      const int col = j * 128 + g * 32 + l31;
      const GAS u16* xp = (const GAS u16*)x + (size_t)(r * 128 + rg * 32 + h * 4) * 512 + col;
      u32 xv[16], dv = ((const GAS u16*)dsk)[col], bv = ((const GAS u16*)b2)[col];
#pragma unroll
      for (int e = 0; e < 16; ++e) xv[e] = xp[((e & 3) + 8 * (e >> 2)) * 512];
      SB;
      waitv<18>();
      sig<C_LD>(sv);
      seen();
      SB;
#pragma unroll
      for (int e = 0; e < 16; ++e) {
        float xf = bf2f(xv[e]), xn = fmaf(xf, rsv[e >> 2][e & 3], nmv[e >> 2][e & 3]);
        pin(xn);
        xf += bf2f(bv);
        pin(xf);
        ep[e] = fmaf(xn, bf2f(dv), xf);
        pin(ep[e]);
      }
    }
    watch(IC<-1>{});
    STAMP(11);
    lwait<C_PAIR + 8, 1>(svp);
    STAMP(12);
    u32x4 xi[6];
    _Pragma("nounroll") for (u32 it = 0; it < SPIN_CAP; ++it) {
#pragma unroll
      for (int i = 0; i < 6; ++i) ldg4(xi[i], l16, rw, xin + i * 1024);
      asm volatile("s_waitcnt vmcnt(0)" : "+v"(xi[0]), "+v"(xi[1]), "+v"(xi[2]), "+v"(xi[3]), "+v"(xi[4]), "+v"(xi[5])::"memory");
      u32 m = 0;
#pragma unroll
      for (int i = 0; i < 6; ++i) m = max(max(max(max(m, xi[i].x), xi[i].y), xi[i].z), xi[i].w);
      if (__builtin_amdgcn_ballot_w64(m == ~0u) == 0) break;
    }
    STAMP(13);
#pragma unroll
    for (int i = 0; i < 6; ++i)
#pragma unroll
      for (int e = 0; e < 4; ++e) {
        ep[(i & 1) * 8 + 2 * e] += bf2f(xi[i][e]);
        ep[(i & 1) * 8 + 2 * e + 1] += bfh(xi[i][e]);
      }
#pragma unroll
    for (int e = 0; e < 16; ++e) pin(ep[e]);
    lwait<C_PAIR + 8, 2>(svp);
    STAMP(14);
    hold(14);
    GAS float* op = (GAS float*)out + (size_t)(r * 128 + rg * 32 + h * 4) * 512 + j * 128 + g * 32 + l31;
#pragma unroll
    for (int q = 0; q < 4; ++q) {
      const f32x4 o = LDS(f32x4, trb + q * 1024 + l16);
#pragma unroll
      for (int k = 0; k < 4; ++k) op[(8 * q + k) * 512] = ep[4 * q + k] + o[k];
    }
    if (r == 15 && s == 0 && w == 0) stg2<0>(u32x2{tlo, thi}, lane == 0 ? 0 : OOB, rw);
    done();
  } else {
    stats();
    f32x16 a0[2] = {}, a1[2];
    bf16x8 xr[32], fw[3][2];
    int wa[2];
    {
      const int aw0 = (8 * h + tq) * 128 + ((tq >> 1) << 6) + (gi & 1) * 32 + tp * 8;
      wa[0] = opq(aw0 + WB);
      wa[1] = opq((aw0 ^ 64) + WB);
    }
    auto rdw = [&](int n, int t) AI {
      if (n == 32) wa[t] = opq((wa[t] + 16384) ^ (j == 1 ? 64 : 0));
      fw[n % 3][t] = ldtr(wa[t] + (n < 32 ? RO[(n >> 2) % 3] + 8192 : (U1S[(n >> 2) & 7] - 8) * 8192) + (n & 3) * 2048, 512);
    };
    auto rdx = [&](int n) AI { xr[n] = ld128(xa[n & 3] + RO[(n >> 2) % 3]); };
    u32x4 rc;
    float sd;
    u32x2 ct[4];
    u32x4 hf[44];
    float gz, gr, go;
    auto gelu = [&](const f32x16* a, int t, int o) AI {
      const int v = t >> 1;
      if (!(t & 1)) {
        if (v < 32) {
          const float x_ = a[v >> 4][v & 15];
          gz = __builtin_amdgcn_exp2f(fmaf(asf(rc.y), x_ * x_, asf(rc.x)) * x_);
          pin(gz);
        }
      } else {
        if (v < 32) gz = fmaf(gz, sd, sd);
        if (v > 0) {
          const float y = a[(v - 1) >> 4][(v - 1) & 15] * gr;
          if (v & 1) {
            go = y;
            pin(go);
          } else {
            u32 p = pk2(go, y);
            pin(p);
            const int i = (v - 2) >> 1, f = i >> 2;
            hf[o ? (f < 2 ? KT + f : 40 + f) : f][i & 3] = p;
          }
        }
        if (v < 32) {
          gr = __builtin_amdgcn_rcpf(gz);
          pin(gr);
        }
      }
    };
    const int hfo = (int)WS_HF + (r * 4 + rg) * 56 * 2048;
    const int hq = hfo + g * 14 * 2048, hso = (int)WS_HS + (r * 4 + rg) * 65536;
    const int fo = opq(lane == 0 ? ffb + j * 128 : OOB);
    SB;
    bar<4>();
    STAMP(3);
    hold(3);
    rdw(0, 1); rdw(0, 0); rdx(0); rdw(1, 1); rdw(1, 0); rdx(1);
    sfor<0, 32>([&](auto Nn) AI {
      constexpr int n = decltype(Nn)::v, ks = n & 3;
      SB;
      a0[0] = MFMA32(fw[n % 3][0], xr[n], a0[0]); pin(a0[0]);
      SB;
      if (ks != 2) {
        rdw(n + 2, 1);
        rdw(n + 2, 0);
      }
      if constexpr (n < 2) PIECE(2, 2 * n);
      SB;
      a0[1] = MFMA32(fw[n % 3][1], xr[n], a0[1]); pin(a0[1]);
      SB;
      if (ks == 2) {
        bar<4>();
        STS(4, 5 + (n >> 2));
        if (n == 30) {
          rc = LDS(u32x4, MI_RC + (rg * 32 + l31) * 16);
          const int a = opq(MI_CT + l31 * 8);
#pragma unroll
          for (int t = 0; t < 4; ++t) ct[t] = LDS(u32x2, a + t * 256);
        }
        rdw(n + 2, 1);
        rdw(n + 2, 0);
      }
      if (n + 2 < 32) rdx(n + 2);
      if constexpr (n < 2) PIECE(2, 2 * n + 1);
      else if constexpr (n < 22) PIECE(((n - 2) >> 2) + 3, (n - 2) & 3);
      else if constexpr (n < 30) w1p(IC<n - 22>{});
    });
    SB;
    PRIO(1);
    {
      const bf16x8 cb = BC(bf16x8, (u32x4{h ? 0u : rc.z, h ? 0u : rc.w, 0u, 0u}));
      sd = bf2f(rc.z);
      a0[0] = MFMA32(BC(bf16x8, (u32x4{ct[0].x, ct[0].y, ct[1].x, ct[1].y})), cb, a0[0]); pin(a0[0]);
      a1[0] = MFMA32(BC(bf16x8, (u32x4{ct[2].x, ct[2].y, ct[3].x, ct[3].y})), cb, f32x16{}); pin(a1[0]);
      const bf16x8 cc = BC(bf16x8, (u32x4{0u, 0u, h ? 0u : rc.z, h ? 0u : rc.w}));
      a0[1] = MFMA32(BC(bf16x8, (u32x4{ct[0].x, ct[0].y, ct[1].x, ct[1].y})), cc, a0[1]); pin(a0[1]);
      a1[1] = MFMA32(BC(bf16x8, (u32x4{ct[2].x, ct[2].y, ct[3].x, ct[3].y})), cc, f32x16{}); pin(a1[1]);
    }
    STAMP(4);
    hold(4);
    f32x16 acc[4] = {};
    bf16x8 wf[2][4];
    u32 pk = 0, pq = 0;
    int a2[2][4], ac[4], ta;
    auto rd2 = [&](int k, int i, int buf) AI {
      if ((k >= KS && k < KS + 4) || (k >= 22 && k < 34)) {
        const int part = k < 10 ? (k - KS) >> 1 : (k < 26 ? (k - 22) >> 1 : 2 + ((k - 26) >> 2));
        const int q = k < 10 ? k & 1 : (k < 26 ? 2 + (k & 1) : (k - 26) & 3);
        wf[buf][i] = ld128(ac[q] + part * 16384 + i * 4096);
      } else {
        const int p = k < 4 ? k >> 1 : (k < 10 ? 2 : (k < 22 ? (k - 4) >> 1 : (k - 16) >> 1));
        const int sl = W2S[p];
        wf[buf][i] = ldtr(a2[sl >= 12][i] + (sl >= 12 ? sl - 12 : sl) * 8192 + (k & 1) * 4096, 2048);
      }
    };
    auto event = [&](auto Ee) AI {
      constexpr int idx = decltype(Ee)::v, kind = EV.e[idx].kind, a = EV.e[idx].arg;
      if constexpr (kind == E_ST) {
        if constexpr (a < 4) stg4<a * 1024>(hf[a], l16, rw, hfo + u0 * 4096);
        else if constexpr (a < 6) stg4<(a - 4) * 1024>(hf[KT + a - 4], l16, rw, hfo + u1 * 4096 + (j == 1 ? 2048 : 0));
        else stg4<(a - 4) * 1024>(hf[36 + a], l16 | (j >= 2 ? 0 : OOB), rw, hfo + u1 * 4096);
      } else if constexpr (kind == E_SS) {
        stg4<a * 1024>(hf[KS + a], l16, rw, hso + s * 4096);
      } else if constexpr (kind == E_FL) {
        waitv<idx - flaglast(a) - 1>();
        stg2<a * 512>(u32x2{tlo, thi}, fo, rw);
      } else if constexpr (a >= 22 && a < 34) {
        constexpr int d = a < 26 ? 1 : 2 + (((a - 26) >> 1) & 1), part = a < 26 ? (a - 22) >> 1 : (a - 26) >> 2;
        ldg4o<part * 2048 + (a & 1) * 1024>(hf[a], l16, rw, hso + (s ^ d) * 4096);
      } else {
        ldg4o<(a & 1) * 1024>(hf[a], l16, rw, hq + tile(a < 22 ? (a - 4) >> 1 : (a - 16) >> 1) * 2048);
      }
    };
    auto hlast = [&]() AI {
      const int so = hq + 13 * 2048, jj = j;
      asm volatile("s_cmp_gt_u32 %[j], 1\n\ts_cbranch_scc1 1f\n\tbuffer_load_dwordx4 %0, %[v], %[r], %[s] offen sc1\n\t"
                   "buffer_load_dwordx4 %1, %[v], %[r], %[s] offen offset:1024 sc1\n1:"
                   : "+v"(hf[42]), "+v"(hf[43])
                   : [v] "v"(l16), [r] "s"(rw), [s] "s"(so), [j] "s"(jj)
                   : "memory", "scc");
    };
    auto fill = [&](auto Gg) AI {
      constexpr int G = decltype(Gg)::v;
#pragma unroll
      for (int t = gsub(G, GA0, GA1); t < gsub(G + 1, GA0, GA1); ++t) gelu(a0, t, 0);
#pragma unroll
      for (int t = gsub(G, GB0, GB1); t < gsub(G + 1, GB0, GB1); ++t) gelu(a1, t, 1);
      sfor<0, 5>([&](auto Cc) AI {
        constexpr int c = decltype(Cc)::v;
        if constexpr (G == chkgap(c)) chk<C_PAIR + 4, c + 1>(svp, pq);
        if constexpr (G == chkgap(c) - 1) pq = peek<C_PAIR + 4>(svp);
      });
      sfor<evfirst(G), evfirst(G + 1)>(event);
      if constexpr (G == chkgap(4) + 4) hlast();
    };
    sfor<32, 64>([&](auto Nn) AI {
      constexpr int n = decltype(Nn)::v, m = n - 32;
      SB;
      a1[0] = MFMA32(fw[n % 3][0], xr[m], a1[0]); pin(a1[0]);
      SB;
      if (m == 14) chk<C_U1, 8>(sv, pk);
      if (n + 2 < 64) rdw(n + 2, 1);
      if (m == 30) chk<C_LA, 4>(sv, pk);
      if (m >= 30) rd2(0, 2 * (m - 30), 0);
      fill(IC<2 * m>{});
      SB;
      a1[1] = MFMA32(fw[n % 3][1], xr[m], a1[1]); pin(a1[1]);
      SB;
      if (n + 2 < 64) rdw(n + 2, 0);
      if (m >= 30) rd2(0, 2 * (m - 30) + 1, 0);
      if (m == 3) {
        waitv<0>();
        sig<C_U1>(sv);
      }
      fill(IC<2 * m + 1>{});
      if (m == 13) pk = peek<C_U1>(sv);
      if (m == 15) sig<C_P1>(sv);
      if (m == 31) sig<C_P2>(sv);
      if (m == 29) pk = peek<C_LA>(sv);
      if (m == 28) {
        const int a = (4 * h + tq) * 256 + (tq << 6) + (gi & 1) * 32 + tp * 8;
#pragma unroll
        for (int i = 0; i < 4; ++i) a2[0][i] = opq(a ^ (i << 6));
      }
      if (m == 29) {
#pragma unroll
        for (int i = 0; i < 4; ++i) a2[1][i] = opq(a2[0][i] + 98304);
      }
      if (m == 30) {
#pragma unroll
        for (int i = 0; i < 4; ++i) ac[i] = opq(ax ^ (i * 32 + 32768) ^ ((j & 1) * 64));
      }
      if (m == 31) ta = opq(trb + (8 * h + tq) * 72 + (16 * (gi & 1) + 4 * tp) * 2);
      if (m == 7) STS(1, 5);
      if (m == 15) STS(1, 6);
      if (m == 23) STS(1, 7);
    });
    STS(0, 5); STS(1, 8);
    sfor<0, 44>([&](auto Kk) AI {
      constexpr int k = decltype(Kk)::v;
      if constexpr (k >= 10 && k < 42) asm volatile("s_waitcnt vmcnt(%1)" : "+v"(hf[k]) : "n"(evfirst(gap1(k)) - evfind(E_LD, k) - 1) : "memory");
      if constexpr (k == 42) asm volatile("s_waitcnt vmcnt(0)" : "+v"(hf[42]), "+v"(hf[43])::"memory");
      sfor<(k >= KD ? 1 : 0), 4>([&](auto Ii) AI {
        constexpr int i = decltype(Ii)::v;
        SB;
        acc[i] = MFMA32(BC(bf16x8, hf[k]), wf[k & 1][i], acc[i]); pin(acc[i]);
        SB;
        if (i == 1) {
          if (k == 12) chk<C_LB, 4>(sv, pk);
          if (k == 24) chk<C_LC, 4>(sv, pk);
          if (k == 32) chk<C_LD, 4>(sv, pk);
          if (k == 43) chk<C_PAIR + 4, 6>(svp, pk);
        }
        if (k + 1 < 44 && (i > 0 || k + 1 < KD)) rd2(k + 1, i, (k + 1) & 1);
        if constexpr (k == 43 && i > 1) {
          rd2(KD + i - 2, 0, 0);
          wf[0][i - 1] = wf[0][0];
        }
        if (k == KS - 1 && i == 1) {
          chk<C_PAIR, 1>(svp, pk);
#pragma unroll
          for (int f = 0; f < 4; ++f) hf[KS + f] = BC(u32x4, ldtr(ta + (f >> 1) * 2304 + (f & 1) * 16 * 72, 4 * 72));
        }
        fill(IC<gapof(k, i)>{});
        if (k == KS - 1 && i == 0) pk = peek<C_PAIR>(svp);
        if (i == 3) {
          if (k == 11) pk = peek<C_LB>(sv);
          if (k == 23) pk = peek<C_LC>(sv);
          if (k == 31) pk = peek<C_LD>(sv);
          if (k == 42) pk = peek<C_PAIR + 4>(svp);
          if (k == 13) sig<C_P3>(sv);
        }
      });
      if (k == 3) STS(1, 9);
      if (k == 5) STS(1, 10);
      if (k == 7) STS(1, 11);
      if (k == 9) STS(1, 12);
      if (k == 10) STS(1, 13);
      if (k == 12) STS(1, 14);
      if (k == 9) STS(0, 6);
      if (k == 15) STS(0, 7);
      if (k == 21) STS(0, 8);
      if (k == 27) STS(0, 9);
      if (k == 33) STS(0, 10);
      if (k == 39) STS(0, 11);
    });
    STS(0, 12);
    u32x4 xo;
    sfor<KD, 44>([&](auto Kk) AI {
      constexpr int k = decltype(Kk)::v, t = k - KD;
      SB;
      acc[0] = MFMA32(BC(bf16x8, hf[k]), wf[0][1 + t % 3], acc[0]); pin(acc[0]);
      SB;
      if (k + 2 < 44) {
        rd2(k + 2, 0, 0);
        wf[0][1 + (t + 2) % 3] = wf[0][0];
      }
      if constexpr (t < 12) {
        constexpr int i = 1 + (t >> 2), e = (t & 3) * 4;
        u32 p0 = min(pk2(acc[i][e], acc[i][e + 1]), 0xfffffffeu), p1 = min(pk2(acc[i][e + 2], acc[i][e + 3]), 0xfffffffeu);
        pin(p0, p1);
        xo[(t & 1) * 2] = p0;
        xo[(t & 1) * 2 + 1] = p1;
        if (t & 1) stg4<((t >> 1) & 1) * 1024>(xo, l16, rw, (int)WS_X + (((r * 4 + j) * 4 + (g ^ i)) * 4 + rg) * 6144 + (i - 1) * 2048);
      }
      if (t == 11) sig<C_PAIR + 8>(svp);
    });
    SB;
    STS(0, 13);
#pragma unroll
    for (int q = 0; q < 4; ++q) LDS(f32x4, trb + q * 1024 + l16) = f32x4{acc[0][4 * q], acc[0][4 * q + 1], acc[0][4 * q + 2], acc[0][4 * q + 3]};
    sig<C_PAIR + 8>(svp);
    done();
  }
}

__global__ __launch_bounds__(512, 2) void fused_ssm_ffn(const u16* __restrict__ x, const u16* __restrict__ w1, const u16* __restrict__ bre,
                                                         const u16* __restrict__ bim, char* __restrict__ ws, const u16* __restrict__ b1,
                                                         const u16* __restrict__ lre, const u16* __restrict__ lim, const u16* __restrict__ ldt,
                                                         const u16* __restrict__ cre, const u16* __restrict__ cim, const u16* __restrict__ w2,
                                                         const u16* __restrict__ dsk, const u16* __restrict__ b2, float* __restrict__ out) {
  u64 stp[1] = {0};
  STAMP(0);
  asm volatile("s_memrealtime %0\n\ts_waitcnt lgkmcnt(0)" : "=s"(stp[0])::"memory");
  const u64 ka = (u64)__builtin_amdgcn_kernarg_segment_ptr();
  u32 j0, j1;
  asm volatile("s_load_dword %0, %2, 0\n\ts_load_dword %1, %2, 64" : "=&s"(j0), "=&s"(j1) : "s"(ka) : "memory");
  const int w = __builtin_amdgcn_readfirstlane(threadIdx.x >> 6);
  if (w < 4) role<true>(w, threadIdx.x, ka, j0, j1, stp[0]);
  else role<false>(w, threadIdx.x, ka, j0, j1, stp[0]);
}

extern "C" void kernel_launch(void* const* d_in, const int* in_sizes, int n_in, void* d_out, int out_size, void* d_ws, size_t ws_size,
                              hipStream_t stream) {
  (void)hipFuncSetAttribute((const void*)fused_ssm_ffn, hipFuncAttributeMaxDynamicSharedMemorySize, LDS_BYTES);
  fused_ssm_ffn<<<256, 512, LDS_BYTES, stream>>>((const u16*)d_in[0], (const u16*)d_in[10], (const u16*)d_in[3], (const u16*)d_in[4], (char*)d_ws,
                                                 (const u16*)d_in[11], (const u16*)d_in[1], (const u16*)d_in[2], (const u16*)d_in[7],
                                                 (const u16*)d_in[5], (const u16*)d_in[6], (const u16*)d_in[12], (const u16*)d_in[8],
                                                 (const u16*)d_in[13], (float*)d_out);
}
```

```cpp
#include <hip/hip_runtime.h>
#include <hip/hip_bf16.h>

typedef unsigned short u16;
typedef unsigned u32;
typedef unsigned long long u64;
typedef short bf16x8 __attribute__((ext_vector_type(8)));
typedef short bf16x4 __attribute__((ext_vector_type(4)));
typedef float f32x16 __attribute__((ext_vector_type(16)));
typedef float f32x4 __attribute__((ext_vector_type(4)));
typedef float f32x2 __attribute__((ext_vector_type(2)));
typedef __bf16 bf16x2n __attribute__((ext_vector_type(2)));
typedef u32 u32x4 __attribute__((ext_vector_type(4)));
typedef u32 u32x2 __attribute__((ext_vector_type(2)));
typedef int i32x4 __attribute__((ext_vector_type(4)));
#define LAS __attribute__((address_space(3)))
#define DI __device__ __forceinline__
#define AI __attribute__((always_inline))
#define SB __builtin_amdgcn_sched_barrier(0)
#define PRIO(n) do { SB; asm volatile("s_setprio " #n); SB; } while (0)
#define MFMA32(a, b, c) __builtin_amdgcn_mfma_f32_32x32x16_bf16((a), (b), (c), 0, 0, 0)
#define BC(T, v) __builtin_bit_cast(T, v)
#define SC1 16

constexpr u32 REC = 2304;
constexpr u32 WS_GR = 4096;
constexpr u32 WS_GS = WS_GR + 131072;
constexpr u32 WS_GB = WS_GS + 16384;
constexpr u32 WS_GW = WS_GB + 8192;
constexpr u32 WS_HF = 524288;
constexpr u32 WS_HS = WS_HF + 64 * 56 * REC;
constexpr u32 WS_X = WS_HS + 64 * 32 * REC;
constexpr u32 WS_END = WS_X + 1024 * 3 * REC;
static_assert(WS_GW + 8 * 1792 * 8 <= WS_HF, "ws map");

constexpr int WB = 49152;
constexpr int RO[3] = {16384, 0, 32768};
constexpr int U1S[8] = {12, 13, 14, 15, 10, 11, 8, 9};
constexpr int W2S[14] = {16, 0, 1, 2, 3, 12, 13, 14, 15, 16, 0, 1, 2, 3};
constexpr int MI_TR = 139264, MI_PW = MI_TR, MI = MI_TR + 8 * 2304;
constexpr int MI_PS = MI, MI_CT = MI + 128, MI_RC = MI_CT + 1024, MI_RS = MI_RC + 2048, MI_NM = MI_RS + 512, MI_WT = MI_NM + 512, MI_HP = MI_WT + 1024,
              MI_CNT = MI_HP, MI_JUNK = MI_CNT + 128;
constexpr int MI_HQ = MI_CT;
constexpr int LDS_BYTES = 163840;
static_assert(MI_JUNK + 256 + 128 <= LDS_BYTES, "lds map");
constexpr int C_WT = 0, C_HP = 4, C_U1 = 8, C_LA = 12, C_LB = 16, C_LC = 20, C_LD = 24, C_P1 = 28, C_P2 = 32, C_P3 = 36, C_PAIR = 64;
constexpr int OOB = 0x40000000;
constexpr u32 SPIN_CAP = 1u << 18;
#define KD 22

#define STAMP(i)
#define SET 0
#define RETRY(k)
#define STS(set, i) do { if (SET == (set)) STAMP(i); } while (0)

DI float asf(u32 b) { return BC(float, b); }
DI u32 asu(float f) { return BC(u32, f); }
DI float bf2f(u32 b) { return asf(b << 16); }
DI float bfh(u32 b) { return asf(b & 0xffff0000u); }
DI u32 pk2(float a, float b) {
  f32x2 v = {a, b};
  return BC(u32, __builtin_convertvector(v, bf16x2n));
}
DI float rbf(float a) { return bf2f(pk2(a, a)); }
DI i32x4 mkrs(u64 a, u32 bytes) {
  i32x4 r = {(int)(u32)a, (int)((u32)(a >> 32) & 0xffffu), (int)bytes, 0x00020000};
  return r;
}
template <int SI, int LI> DI void dma1(i32x4 rs, int v, int sb, int lw) {
  int t;
  asm volatile("s_add_i32 m0, %[lw], %[li]\n\ts_add_i32 %[t], %[sb], %[si]\n\tbuffer_load_dwordx4 %[v], %[rs], %[t] offen lds"
               : [t] "=&s"(t)
               : [v] "v"(v), [rs] "s"(rs), [sb] "s"(sb), [lw] "s"(lw), [si] "n"(SI), [li] "n"(LI)
               : "memory", "scc");
}
template <int LI> DI void dma_s(i32x4 rs, int v, int& so, int ps, int lw) {
  asm volatile("s_add_i32 m0, %[lw], %[li]\n\ts_add_i32 %[t], %[t], %[ps]\n\tbuffer_load_dwordx4 %[v], %[rs], %[t] offen lds"
               : [t] "+s"(so)
               : [v] "v"(v), [rs] "s"(rs), [ps] "s"(ps), [lw] "s"(lw), [li] "n"(LI)
               : "memory", "scc");
}
DI void touch(const void* p, int lw) {
  asm volatile("s_mov_b32 m0, %1\n\ts_nop 0\n\tglobal_load_lds_dword %0, off" ::"v"(p), "s"(lw) : "memory");
}
template <int I> struct IC { static constexpr int v = I; };
template <int A, int B, class Fn> DI void sfor(Fn&& f) {
  if constexpr (A < B) {
    f(IC<A>{});
    sfor<A + 1, B>(f);
  }
}
typedef f32x2 cf;
template <class T> DI void pin1(T& v) { asm volatile("" : "+v"(v)); }
template <class... T> DI void pin(T&... v) { (pin1(v), ...); }
DI int opq(int v) { asm volatile("" : "+v"(v)); return v; }
DI int opqs(int v) { asm volatile("" : "+s"(v)); return v; }
template <int N> DI void bar() { asm volatile("s_waitcnt vmcnt(%0) lgkmcnt(0)\n\ts_barrier" ::"n"(N) : "memory"); }
template <int N> DI void waitv() { asm volatile("s_waitcnt vmcnt(%0)" ::"n"(N) : "memory"); }
DI void waitl() { asm volatile("s_waitcnt lgkmcnt(0)" ::: "memory"); }
DI bf16x8 ld128(int a) { return *(LAS bf16x8*)(size_t)a; }
DI bf16x8 ldtr(int a, int d) {
  bf16x4 lo = __builtin_amdgcn_ds_read_tr16_b64_v4i16((LAS bf16x4*)(size_t)a);
  bf16x4 hi = __builtin_amdgcn_ds_read_tr16_b64_v4i16((LAS bf16x4*)(size_t)(a + d));
  return __builtin_shufflevector(lo, hi, 0, 1, 2, 3, 4, 5, 6, 7);
}
#define LDS(T, a) (*(LAS T*)(size_t)(a))
DI float add32(float a, float b) {
  auto r = __builtin_amdgcn_permlane32_swap(asu(a), asu(b), false, false);
  return asf(r[0]) + asf(r[1]);
}
DI float add16(float a, float b) {
  auto r = __builtin_amdgcn_permlane16_swap(asu(a), asu(b), false, false);
  return asf(r[0]) + asf(r[1]);
}
template <int C> DI float dppadd(float v) { return v + asf((u32)__builtin_amdgcn_update_dpp(0, (int)asu(v), C, 0xf, 0xf, true)); }
DI float rsum(float v) {
  v = dppadd<0xB1>(v);
  v = dppadd<0x4E>(v);
  v = dppadd<0x141>(v);
  return dppadd<0x140>(v);
}
DI void halves(float v, float& a, float& b) {
  auto r = __builtin_amdgcn_permlane32_swap(asu(v), asu(v), false, false);
  a = asf(r[0]);
  b = asf(r[1]);
}
DI u32 split2(float c) {
  return pk2(c, c - rbf(c));
}
DI float dot2(u32 a, u32 b, float c) { return __builtin_amdgcn_fdot2_f32_bf16(BC(bf16x2n, a), BC(bf16x2n, b), c, false); }
DI cf pfma(cf a, cf b, cf c) {
  asm("v_pk_fma_f32 %0, %1, %2, %0 op_sel_hi:[0,1,1]\n\tv_pk_fma_f32 %0, %1, %2, %0 op_sel:[1,1,0] op_sel_hi:[1,0,1] neg_lo:[1,0,0]"
      : "+&v"(c)
      : "v"(a), "v"(b));
  return c;
}
DI cf pmul(cf a, cf b) {
  cf t;
  asm("v_pk_mul_f32 %0, %1, %2 op_sel_hi:[0,1]\n\tv_pk_fma_f32 %0, %1, %2, %0 op_sel:[1,1,0] op_sel_hi:[1,0,1] neg_lo:[1,0,0]"
      : "=&v"(t)
      : "v"(a), "v"(b));
  return t;
}
DI cf csq(cf a) { return pmul(a, a); }
DI float sfma(float a, float b, float c) { asm("v_fma_f32 %0, %1, %2, %3" : "=v"(c) : "v"(a), "v"(b), "v"(c)); return c; }
DI cf cfma(cf a, cf b, cf c) {
  float x = c.x, y = c.y;
  asm("v_fmac_f32 %0, %2, %4\n\tv_fmac_f32 %1, %2, %5\n\tv_fma_f32 %0, -%3, %5, %0\n\tv_fmac_f32 %1, %3, %4"
      : "+&v"(x), "+&v"(y)
      : "v"(a.x), "v"(a.y), "v"(b.x), "v"(b.y));
  return cf{x, y};
}
DI cf cfma3(cf a, cf b, cf c) {
  float x, y;
  asm("v_fma_f32 %0, %2, %4, %6\n\tv_fma_f32 %1, %2, %5, %7\n\tv_fma_f32 %0, -%3, %5, %0\n\tv_fmac_f32 %1, %3, %4"
      : "=&v"(x), "=&v"(y)
      : "v"(a.x), "v"(a.y), "v"(b.x), "v"(b.y), "v"(c.x), "v"(c.y));
  return cf{x, y};
}
DI cf cmul(cf a, cf b) {
  float x, y;
  asm("v_mul_f32 %0, %2, %4\n\tv_mul_f32 %1, %2, %5\n\tv_fma_f32 %0, -%3, %5, %0\n\tv_fmac_f32 %1, %3, %4"
      : "=&v"(x), "=&v"(y)
      : "v"(a.x), "v"(a.y), "v"(b.x), "v"(b.y));
  return cf{x, y};
}
DI cf csel(bool c, cf a, cf b) { return cf{c ? a.x : b.x, c ? a.y : b.y}; }
DI void settle(cf& a) { asm volatile("s_nop 1" : "+v"(a)); }
template <int O> DI void sig(int sv) { asm volatile("ds_inc_u32 %0, %0 offset:%1" ::"v"(sv), "n"(O) : "memory"); }
template <int O> DI u32 peek(int sv) { return *(volatile LAS u32*)(size_t)(sv + O); }
template <int O, int N> DI void chk(int sv, u32 v) {
  int t, c;
  asm volatile("v_readfirstlane_b32 %[t], %[v]\n\ts_cmp_ge_u32 %[t], %[n]\n\ts_cbranch_scc1 2f\n\ts_mov_b32 %[c], 0x400000\n"
               "1:\n\ts_sleep 1\n\tds_read_b32 %[v], %[a] offset:%[o]\n\ts_sub_u32 %[c], %[c], 1\n\ts_waitcnt lgkmcnt(0)\n\ts_cbranch_scc1 2f\n\t"
               "v_readfirstlane_b32 %[t], %[v]\n\ts_cmp_lt_u32 %[t], %[n]\n\ts_cbranch_scc1 1b\n2:"
               : [t] "=&s"(t), [c] "=&s"(c), [v] "+v"(v)
               : [a] "v"(sv), [o] "n"(O), [n] "n"(N)
               : "memory", "scc");
}
template <int O, int N> DI void lwait(int sv) { chk<O, N>(sv, peek<O>(sv)); }
DI void ldg4(u32x4& d, int vo, i32x4 rs, int so) {
  asm volatile("buffer_load_dwordx4 %0, %1, %2, %3 offen sc1" : "=v"(d) : "v"(vo), "s"(rs), "s"(so) : "memory");
}
template <int O> DI void ldg4o(u32x4& d, int vo, i32x4 rs, int so) {
  asm volatile("buffer_load_dwordx4 %0, %1, %2, %3 offen offset:%4 sc1" : "=v"(d) : "v"(vo), "s"(rs), "s"(so), "n"(O) : "memory");
}
DI void ldg1(u32& d, int vo, i32x4 rs, int so) {
  asm volatile("buffer_load_dword %0, %1, %2, %3 offen offset:2048 sc1" : "=v"(d) : "v"(vo), "s"(rs), "s"(so) : "memory");
}
DI void stg1(u32 d, int vo, i32x4 rs, int so) {
  asm volatile("buffer_store_dword %0, %1, %2, %3 offen offset:2048 sc1" ::"v"(d), "v"(vo), "s"(rs), "s"(so) : "memory");
}
DI u32 csum(u32x4 d, u32 c) { return (d.w << 3) + ((d.z << 2) + ((d.y << 1) + (d.x + c))); }
DI void refetch(u32x4& a, u32x4& b, int l16, i32x4 rs, int so, u32 tag) {
  _Pragma("nounroll") for (u32 it = 0; it < SPIN_CAP; ++it) {
    u32 c;
    asm volatile("buffer_load_dwordx4 %0, %3, %5, %6 offen sc1\n\tbuffer_load_dwordx4 %1, %3, %5, %6 offen offset:1024 sc1\n\t"
                 "buffer_load_dword %2, %4, %5, %6 offen offset:2048 sc1\n\ts_waitcnt vmcnt(0)"
                 : "=&v"(a), "=&v"(b), "=&v"(c)
                 : "v"(l16), "v"(l16 >> 2), "s"(rs), "s"(so)
                 : "memory");
    if (__builtin_amdgcn_ballot_w64(csum(b, csum(a, tag)) != c) == 0) break;
  }
}
DI void ldg2(u32x2& d, int vo, i32x4 rs) { asm volatile("buffer_load_dwordx2 %0, %1, %2, 0 offen sc1" : "=v"(d) : "v"(vo), "s"(rs) : "memory"); }
template <int O> DI void stg4(u32x4 d, int vo, i32x4 rs, int so) {
  asm volatile("buffer_store_dwordx4 %0, %1, %2, %3 offen offset:%4 sc1" ::"v"(d), "v"(vo), "s"(rs), "s"(so), "n"(O) : "memory");
}
template <int O> DI void stg2(u32x2 d, int vo, i32x4 rs) {
  asm volatile("buffer_store_dwordx2 %0, %1, %2, 0 offen offset:%3 sc1" ::"v"(d), "v"(vo), "s"(rs), "n"(O) : "memory");
}

constexpr int aextra(int n) { return n == 17 ? 4 : ((n == 1 || n == 21) ? 1 : 0); }
constexpr int abar(int nb) {
  int c = (nb >= 6 && nb != 22) ? aextra(nb - 5) : 0;
  for (int m = nb < 6 ? 0 : nb - 4; m < nb; ++m) c += (m < 2 ? 2 : 1) + aextra(m);
  return c;
}
#define KS 6
#define KT 4
#define GA0 0
#define GA1 56
#define GB0 66
#define GB1 101
#define LD0 24
#define LD1 24
#define LD2 24
#define LD3 24
#define LD4 24
constexpr int gapof(int k, int i) { return 64 + (k < KD ? 4 * k + i : 4 * KD + 3 * (k - KD) + i - 1); }
constexpr int gap1(int k) { return gapof(k, k < KD ? 0 : 1); }
constexpr int gsub(int g, int g0, int g1) { return g <= g0 ? 0 : (g >= g1 ? 66 : ((g - g0) * 66 + (g1 - g0) - 1) / (g1 - g0)); }
constexpr int gdone(int t, int g0, int g1) { int g = g0; while (gsub(g + 1, g0, g1) <= t) ++g; return g; }
enum { E_ST, E_LD };
struct Ev { int gap, kind, arg; };
struct EvTab { Ev e[80]; int n; };
constexpr int lead(int k) { return k < 16 ? LD0 : (k < 22 ? LD1 : (k < 34 ? LD2 : (k < 40 ? LD3 : LD4))); }
constexpr EvTab mkev() {
  EvTab t{};
  for (int a = 0; a < 12; ++a) t.e[t.n++] = Ev{(a < 6 ? gdone(32 * (a / 3) + 33, GA0, GA1) : gdone(32 * (a / 3 - 2) + 33, GB0, GB1)) - 1 + a % 3, E_ST, a};
  for (int k = 10; k < 44; k += 2)
    for (int a = 0; a < 3; ++a) t.e[t.n++] = Ev{gap1(k - 1) - lead(k) + 2 * a - (a == 2), E_LD, k / 2 * 3 + a};
  for (int a = 1; a < t.n; ++a)
    for (int c = a; c > 0 && t.e[c - 1].gap > t.e[c].gap; --c) {
      const Ev x = t.e[c];
      t.e[c] = t.e[c - 1];
      t.e[c - 1] = x;
    }
  return t;
}
constexpr EvTab EV = mkev();
constexpr int evfirst(int g) { int a = 0; while (a < EV.n && EV.e[a].gap < g) ++a; return a; }
constexpr int evfind(int kind, int arg) { int a = 0; while (EV.e[a].kind != kind || EV.e[a].arg != arg) ++a; return a; }
static_assert(gdone(33, GA0, GA1) < gap1(1) && gdone(65, GA0, GA1) < gap1(3) && GA1 <= GB0, "first unit");
static_assert(gdone(17, GB0, GB1) < gap1(KT) && gdone(33, GB0, GB1) < gap1(KT + 1) && GB0 >= 65, "second unit");
static_assert(KD >= 14 && KD <= 32, "late steps");

typedef u32 u32x16 __attribute__((ext_vector_type(16)));
typedef u32 u32x8 __attribute__((ext_vector_type(8)));
#define GAS __attribute__((address_space(1)))
template <class V> DI u64 kp(V v, int i) { return (u64)v[2 * i] | ((u64)v[2 * i + 1] << 32); }

template <bool ISA> DI void role(const int w, int tid, const u64 ka, const u32 j0, const u32 j1, const u64 tstart) {
  u32x16 k0;
  u32x8 k1;
  u32x4 k2;
  u32x2 k3;
  asm volatile("s_load_dwordx16 %0, %1, 0" : "=s"(k0) : "s"(ka) : "memory");
  if constexpr (ISA)
    asm volatile("s_load_dwordx8 %0, %2, 64\n\ts_load_dwordx4 %1, %2, 96" : "=&s"(k1), "=&s"(k2) : "s"(ka) : "memory");
  else
    asm volatile("s_load_dwordx2 %0, %1, 112" : "=s"(k3) : "s"(ka) : "memory");
  if constexpr (ISA) asm volatile("; A" : "+v"(tid));
  else asm volatile("; B" : "+v"(tid));
  const int lane = tid & 63;
  const int b = blockIdx.x, xcd = b & 7, q8 = b >> 3;
  const int g = xcd & 3, r = (xcd >> 2) * 8 + (q8 >> 2), j = q8 & 3, s = 4 * g + j;
  const int u0 = 7 * g + ((0x5320 >> (4 * j)) & 15), u1 = 7 * g + ((0x6411 >> (4 * j)) & 15);
  const int rg = w & 3, h = lane >> 5, l31 = lane & 31;
  const int rl = lane >> 3, pos = lane & 7, gi = lane >> 4;
  const int blk = r * 4 + j;
  const int l16 = lane * 16;

  int ox = (r * 128 + s * 8 + w) * 1024 + l16;
  int ow = ((blk & 7) * 64 + w * 8 + rl) * 3584 + (7 * g + (blk < 56 ? blk >> 3 : 0)) * 128 + pos * 16;
  int ob = (s * 32 + r * 2 + (rg >> 1)) * 1024 + l16;
  const int ctc = ((rg >> 1) ? u1 : u0) * 64 + ((rg & 1) ^ (rg >> 1 & (j == 1))) * 32;
  int ol = (s * 32 + l31) * 2, od = (ctc + pos * 4 + gi) * 2;
  const int vxE = rl * 1024 + ((pos ^ (rl >> 1)) << 4), vxO = vxE ^ 64;
  const int vw1 = rl * 3584 + ((pos ^ ((rl & 2) << 1)) << 4);
  const bool xt = ISA || w < 6;
  int vmE = xt ? vxE : vw1, vmO = xt ? vxO : vw1;
  const int pstr = xt ? 8192 : 8 * 3584, cadv = xt ? 128 - 3 * 8192 : 40 * 3584;
  const int mb1 = ISA ? 0 : opqs(s * 32768), mb2 = ISA ? 0 : opqs((w - 6) * 32 * 3584 + u0 * 128);
  int mso = opqs((ISA ? (r * 128 + w * 32) * 1024 : (xt ? mb1 : mb2)) - cadv);
  const int lw4 = opqs(rg * 4096 + (ISA ? 0 : WB));
  const u32 msz = ISA ? 2048u * 1024u : opqs(xt ? 512u * 1024u : 512u * 3584u);
  u32x4 ax_, aw_, ab_ = {};
  u32 pa, pb, pc, pd;
  pin(ox, ow, vmE, vmO);
  if constexpr (ISA) pin(ob, ol, od);
  SB;
  if constexpr (ISA) asm volatile("s_waitcnt lgkmcnt(0)" : "+s"(k0), "+s"(k1), "+s"(k2) : "s"(j0), "s"(j1) : "memory");
  else asm volatile("s_waitcnt lgkmcnt(0)" : "+s"(k0), "+s"(k3) : "s"(j0), "s"(j1) : "memory");
  const u64 x = kp(k0, 0), w1 = kp(k0, 1), bre = kp(k0, 2), bim = kp(k0, 3), ws = kp(k0, 4), b1 = kp(k0, 5), lre = kp(k0, 6), lim = kp(k0, 7);
  const u64 ldt = kp(k1, 0), cre = kp(k1, 1), cim = kp(k1, 2), w2 = kp(k1, 3), dsk = kp(k2, 0), b2 = kp(k2, 1), out = kp(k3, 0);
  if constexpr (ISA) {
    asm volatile("global_load_ushort %0, %1, %2" : "=v"(pa) : "v"(ol), "s"(lre) : "memory");
    asm volatile("global_load_ushort %0, %1, %2" : "=v"(pb) : "v"(ol), "s"(lim) : "memory");
    asm volatile("global_load_ushort %0, %1, %2" : "=v"(pc) : "v"(ol), "s"(ldt) : "memory");
    asm volatile("global_load_ushort %0, %1, %2" : "=v"(pd) : "v"(od), "s"(b1) : "memory");
    asm volatile("global_load_dwordx4 %0, %1, %2" : "=v"(ab_) : "v"(ob), "s"((w & 1) ? bim : bre) : "memory");
  }
  asm volatile("global_load_dwordx4 %0, %1, %2" : "=v"(ax_) : "v"(ox), "s"(x) : "memory");
  asm volatile("global_load_dwordx4 %0, %1, %2" : "=v"(aw_) : "v"(ow), "s"(w1) : "memory");
  const i32x4 rm = mkrs(ISA ? x : (w == 4 ? bre : (w == 5 ? bim : w1)), msz);
#define PIECE(c, p) dma_s<RO[(c) % 3] + (p) * 1024>(rm, ((p) & 1) ? vmO : vmE, mso, (p) == 0 ? cadv : pstr, lw4)
  PIECE(0, 0); PIECE(0, 1); PIECE(0, 2); PIECE(0, 3);
  u32 tag;
  asm volatile("s_load_dword %0, %1, 0" : "=s"(tag) : "s"(ws) : "memory");
  const i32x4 rw = mkrs(ws, WS_END);
  const int sv = opq(lane == 0 ? MI_CNT : MI_JUNK + lane * 4);
  if constexpr (!ISA) LDS(u32, MI_CNT + l31 * 4) = 0u;
  const int ax = opq(l31 * 128 + (((h ^ (l31 >> 1)) & 7) << 4));
  const int tq = (lane >> 2) & 3, tp = lane & 3;
  int xa[4];
#pragma unroll
  for (int k = 0; k < 4; ++k) xa[k] = opq((ax ^ (k * 32)) + rg * 4096);
  const u32 ordl = opqs((j & 2) ? ((j & 1) ? 0x51640CBAu : 0x51A40876u) : ((j & 1) ? 0x71A60354u : 0x75A64210u));
  const u32 ordh = opqs((j & 2) ? ((j & 1) ? 0xD98327u : 0x9DC32Bu) : ((j & 1) ? 0xD9C82Bu : 0xD9C83Bu));
  auto hold = [&](int at) AI {
  };
  auto done = [&]() AI {
    hold(15);
  };
  u32 tlo;
  auto stats = [&]() AI {
    asm volatile("s_waitcnt vmcnt(4) lgkmcnt(0)" : "+v"(ax_), "+v"(aw_), "+s"(tag)::"memory");
    if constexpr (ISA) pin(ab_);
    tlo = opqs(tag + 1u);
    STAMP(1);
    PIECE(1, 0);
    {
      float sm = 0.f, sq = 0.f, bs = 0.f, y[8];
#pragma unroll
      for (int i = 0; i < 4; ++i) {
        sm = dot2(ax_[i], 0x3f803f80u, sm);
        sq = dot2(ax_[i], ax_[i], sq);
        if constexpr (ISA) bs = dot2(ab_[i], 0x3f803f80u, bs);
      }
      sm = add32(sm, sq);
      sm = add16(sm, ISA ? add32(bs, bs) : sm);
      pin(sm);
      SB;
      PIECE(1, 1);
#pragma unroll
      for (int i = 0; i < 4; ++i) {
        y[2 * i] = bf2f(aw_[i]);
        y[2 * i + 1] = bfh(aw_[i]);
      }
      LDS(float, MI_PS + w * 16 + gi * 4) = rsum(sm);
#pragma unroll
      for (int i = 0; i < 4; ++i) y[i] = add32(y[i], y[i + 4]);
      pin(y[0], y[1], y[2], y[3]);
      SB;
      PIECE(1, 2);
      const float z0 = dppadd<0x128>(add16(y[0], y[2])), z1 = dppadd<0x128>(add16(y[1], y[3]));
      LDS(f32x2, MI_PW + w * 256 + (pos * 8 + gi * 2) * 4) = f32x2{z0, z1};
      SB;
      PIECE(1, 3);
    }
    STAMP(2);
  };

  const i32x4 r1 = mkrs(w1, 512u * 3584u);
  const int w1b = w * 8 * 3584 + u1 * 128, lw1 = w * 1024;
  auto w1p = [&](auto Cc) AI {
    constexpr int c = decltype(Cc)::v;
    dma1<c * 64 * 3584, U1S[c] * 8192>(r1, vw1, w1b, lw1);
  };
  auto tile = [&](int p) AI { return (int)((opqs(p >= 8 ? ordh : ordl) >> (4 * (p & 7))) & 15); };
  const int trb = MI_TR + rg * 4608;
  const int svp = sv + rg * 16;

  if constexpr (ISA) {
    cf l1, l2, l3, l4, l8, l32, l256, co2, lF, lL, lH, mq[4];
    {
      asm volatile("s_waitcnt vmcnt(7)" : "+v"(pa), "+v"(pb), "+v"(pc), "+v"(pd)::"memory");
      const float lr = bf2f(pa), li = bf2f(pb), dt = rbf(expf(bf2f(pc))), er = expf(dt * lr), th = dt * li;
      const float n = __builtin_rintf(th * 0.636619772f);
      const float y = fmaf(n, 1.71512451e-15f, fmaf(n, 4.37113883e-8f, fmaf(n, -1.57079637f, th))), z = y * y;
      const float pc_ = fmaf(z * z, fmaf(z, fmaf(z, 2.44331571e-5f, -1.38873163e-3f), 4.16666457e-2f), fmaf(z, -0.5f, 1.0f));
      const float ps_ = fmaf(z * y, fmaf(z, fmaf(z, -1.95152959e-4f, 8.33216087e-3f), -1.66666546e-1f), y);
      const u32 qd = (u32)(int)n;
      float sn = asf(asu((qd & 1) ? pc_ : ps_) ^ ((qd & 2) << 30)), cs = asf(asu((qd & 1) ? ps_ : pc_) ^ (((qd + 1) & 2) << 30));
      if (__builtin_expect(__builtin_amdgcn_ballot_w64(!(fabsf(th) < 8192.f)) != 0, 0)) sincosf(th, &sn, &cs);
      l1 = cf{er * cs, er * sn};
      const float den = 2.0f / fmaf(lr, lr, li * li);
      co2 = pmul(cf{l1.x - 1.0f, l1.y}, cf{lr * den, -li * den});
      const cf one = {1.f, 0.f};
      l2 = csq(l1);
      l3 = pmul(l2, l1);
      l4 = csq(l2);
      l8 = csq(l4);
      lH = csel(h, one, l4);
      l32 = csq(csq(l8));
      const cf l64 = csq(l32), l128 = csq(l64);
      l256 = csq(l128);
      const cf l512 = csq(l256);
      lF = pmul(pmul(csel(rg & 1, l512, one), csel(rg & 2, csq(l512), one)), csel(h, l128, one));
      lL = pmul(csel(rg & 1, l32, one), csel(rg & 2, l64, one));
      mq[0] = pmul(co2, csel(h, l4, one));
      mq[1] = pmul(mq[0], l8);
      mq[2] = pmul(mq[1], l8);
      mq[3] = pmul(mq[2], l8);
      pin(l1, l2, l3, l4, l8, lH, l32, l256, co2, lF, lL, mq[0], mq[1], mq[2], mq[3]);
    }
    stats();
    f32x16 are = {}, aim = {};
    bf16x8 fxr[3], fbr[3], fbi[3];
    int xb[4];
#pragma unroll
    for (int k = 0; k < 4; ++k) xb[k] = opq((ax ^ (k * 32)) + WB);
    auto rdi = [&](int n) AI { fbi[n % 3] = ld128(xb[n & 3] + RO[(n >> 2) % 3] + 4096); };
    auto rdx = [&](int n) AI {
      fbr[n % 3] = ld128(xb[n & 3] + RO[(n >> 2) % 3]);
      fxr[n % 3] = ld128(xa[n & 3] + RO[(n >> 2) % 3]);
    };
    const i32x4 rcr = mkrs(cre, 512u * 1024u), rci = mkrs(cim, 512u * 1024u), r2 = mkrs(w2, 1792u * 1024u);
    const int lw2 = rg * 2048, w2b = (448 * g + rg * 8) * 1024 + j * 256;
    int vw2;
    auto w2t = [&](auto Pp) AI {
      constexpr int p = decltype(Pp)::v;
      const int sb = w2b + (tile(p) << 15);
      dma1<0, W2S[p] * 8192>(r2, vw2, sb, lw2);
      dma1<4096, W2S[p] * 8192 + 1024>(r2, vw2, sb, lw2);
    };
    const int cb0 = opqs((j * 128 + (((rg >> 1) ^ g) * 32) + (rg & 1) * 16) * 1024 + g * 256 + (j >> 1) * 128);
    auto ctile = [&](auto Pp) AI {
      constexpr int p = decltype(Pp)::v;
      const int sb = opqs(cb0) ^ ((p & 1) * 65536 + (p >> 2) * 128);
      dma1<0, (4 + p) * 8192>((p & 2) ? rci : rcr, vxE, sb, lw2);
      dma1<8192, (4 + p) * 8192 + 1024>((p & 2) ? rci : rcr, vxO, sb, lw2);
    };
    float t0, t1;
    int o1, o2, o3, a8, aw, at, lo0, lo1, ap, aq, ta;
    u32x2 gs;
    u32x4 gw0, gw1, gb;
    u32 nm;
    f32x4 rsv[4], nmv[4];
    const char* tch;
    const u64 csrc = (u64)((rg & 1) ? cim : cre);
    const int d0 = r - 1 - 4 * rg - h;
    const int gro = opqs((int)WS_GR + (s * 16 + r) * 512), grs = opqs((int)WS_GR + s * 8192);
    int cvo;
    auto gather = [&]() AI {
      asm volatile("buffer_load_dwordx2 %0, %4, %7, 0 offen sc1\n\tbuffer_load_dwordx4 %1, %5, %7, 0 offen sc1\n\t"
                   "buffer_load_dwordx4 %2, %5, %7, 0 offen offset:16 sc1\n\tbuffer_load_dwordx4 %3, %6, %7, 0 offen sc1"
                   : "=&v"(gs), "=&v"(gw0), "=&v"(gw1), "=&v"(gb)
                   : "v"(o1), "v"(o2), "v"(o3), "s"(rw)
                   : "memory");
    };
    auto side = [&](auto Nn) AI {
      constexpr int n = decltype(Nn)::v;
      if constexpr (n == 1) {
        u32 val = 0;
        int o = OOB;
        if (w == 2) {
          const int a = opq(MI_PW + lane * 4);
          float c[8];
#pragma unroll
          for (int i = 0; i < 8; ++i) c[i] = LDS(float, a + i * 256);
          o = (blk < 56 ? (int)WS_GW + ((blk & 7) * 1792 + (7 * g + (blk >> 3)) * 64) * 8 : OOB) + lane * 8;
          val = asu(((c[0] + c[1]) + (c[2] + c[3])) + ((c[4] + c[5]) + (c[6] + c[7])));
        } else if (w == 3) {
          const f32x4 p = LDS(f32x4, MI_PS + pos * 16);
          const int oa = opqs((int)WS_GS + (r * 128 + s * 8) * 8), oc = opqs((int)WS_GB + (s * 32 + r * 2) * 16 - 64);
          o = (lane < 8 ? oa : (lane < 12 ? oc : OOB)) + lane * 8;
          const float m = p.x * (1.0f / 512.0f), v = fmaxf(fmaf(-m, m, p.z * (1.0f / 512.0f)), 0.f);
          val = lane < 8 ? pk2(m, __builtin_amdgcn_sqrtf(rbf(rbf(v) + 1e-5f))) : split2(p.y);
        }
        stg2<0>(u32x2{val, tlo}, o, rw);
      } else if constexpr (n == 4) {
        o1 = opq((int)WS_GS + (r * 128 + rg * 32 + l31) * 8);
        o2 = opq((int)WS_GW + (rl * 1792 + ctc + pos * 4) * 8);
        o3 = opq((int)WS_GB + (s * 32 + l31) * 16);
      } else if constexpr (n == 5) {
        vw2 = opq(gi * 1024 + (((lane & 15) ^ ((gi ^ g) << 2)) << 4));
        a8 = opq(MI_CT + l31 * 8);
        cvo = opq(lane < (w == 3 ? 32 : 0) ? l16 : OOB);
      } else if constexpr (n == 7) {
        aw = opq(MI_CT + rg * 256 + l31 * 8 + h * 4);
        at = opq(trb + l31 * 72 + h * 8);
      } else if constexpr (n == 8) {
        lo0 = opq(d0 >= 0 ? d0 * 512 + l31 * 16 : OOB);
        lo1 = opq(d0 >= 2 ? (d0 - 2) * 512 + l31 * 16 : OOB);
      } else if constexpr (n == 9) {
        const bool ln = lane < 14;
        const int oa = (g * 3584 + blk * 56 + rg * 14 + lane) * 128, oc = (blk * 8 + (rg >> 1) * 4 + (pos >> 1)) * 1024 + g * 256 + (pos & 1) * 128;
        const int o = ln ? oa : oc;
        const u64 p2 = (u64)w2, p3 = csrc;
        tch = (const char*)(ln ? p2 : p3) + o;
        pin(tch);
      } else if constexpr (n == 11) {
        ap = opq(trb + lane * 8);
        aq = opq(trb + h * 512 + (lane ^ 32) * 8);
        LDS(f32x2, ap) = f32x2{0.f, 0.f};
      } else if constexpr (n == 12) {
        ta = opq(trb + (8 * h + tq) * 72 + (16 * (gi & 1) + 4 * tp) * 2);
      } else if constexpr (n == 17) {
        gather();
      } else if constexpr (n == 21) {
        touch(tch, MI_JUNK);
      } else if constexpr (n == 22) {
        pin(gs, gw0, gw1, gb);
        _Pragma("nounroll") for (u32 it = 0; it < SPIN_CAP; ++it) {
          const u32 d = ((gs.y ^ tlo) | (gw0.y ^ tlo)) | ((gw0.w ^ tlo) | (gw1.y ^ tlo)) | ((gw1.w ^ tlo) | (gb.y ^ tlo)) | (gb.w ^ tlo);
          if (__builtin_expect(__builtin_amdgcn_ballot_w64(d != 0) == 0, 1)) break;
          gather();
          asm volatile("s_waitcnt vmcnt(0)" : "+v"(gs), "+v"(gw0), "+v"(gw1), "+v"(gb)::"memory");
        }
      } else if constexpr (n == 23) {
        t0 = dppadd<0x128>(add16(add32(asf(gw0.x), asf(gw1.x)), add32(asf(gw0.z), asf(gw1.z))));
        pin(t0);
      } else if constexpr (n == 24) {
        const u32 p = split2(t0);
        LDS(u32x2, MI_CT + (rg * 32 + pos * 4 + gi) * 8) = u32x2{pd | (p << 16), p >> 16};
      } else if constexpr (n == 25) {
        t0 = bfh(gs.x);
        t1 = __builtin_amdgcn_rcpf(t0);
        nm = (gs.x & 0xffffu) ^ 0x8000u;
        t1 = fmaf(fmaf(-t0, t1, 1.0f), t1, t1);
        pin(t1, nm);
      } else if constexpr (n == 26) {
        const float gc0 = -2.302208198f * t1;
        LDS(u32x4, MI_RC + (rg * 32 + l31) * 16) = u32x4{asu(gc0), asu(gc0 * 0.044715f * (t1 * t1)), (gs.x >> 16) | (nm << 16), nm};
      } else if constexpr (n == 27) {
        LDS(float, MI_RS + h * 512 + (rg * 32 + l31) * 4) = h ? bf2f(nm) * t1 : t1;
        nm = h ? 0u : nm | (nm << 16);
        pin(nm);
      } else if constexpr (n == 28) {
        are = MFMA32(BC(bf16x8, (u32x4{nm, 0u, 0u, 0u})), BC(bf16x8, (u32x4{gb.x, 0u, 0u, 0u})), are); pin(are);
        aim = MFMA32(BC(bf16x8, (u32x4{nm, 0u, 0u, 0u})), BC(bf16x8, (u32x4{gb.z, 0u, 0u, 0u})), aim); pin(aim);
      } else if constexpr (n == 29) {
        const int a = opq(MI_RS + (rg * 32 + h * 4) * 4);
#pragma unroll
        for (int q = 0; q < 4; ++q) {
          rsv[q] = LDS(f32x4, a + q * 32);
          nmv[q] = LDS(f32x4, a + 512 + q * 32);
        }
      }
    };
    SB;
    bar<4>();
    STAMP(3);
    hold(3);
    rdi(0); rdx(0); rdi(1); rdx(1);
    sfor<0, 32>([&](auto Nn) AI {
      constexpr int n = decltype(Nn)::v, c = n >> 2, ks = n & 3;
      SB;
      are = MFMA32(fxr[n % 3], fbr[n % 3], are); pin(are);
      SB;
      if (ks != 2 && n + 2 < 32) rdi(n + 2);
      if constexpr (n < 2) PIECE(2, 2 * n);
      if constexpr (ks != 2) {
        if constexpr (n < 2) PIECE(2, 2 * n + 1);
        else if constexpr (n < 22) PIECE(((n - 2) >> 2) + 3, (n - 2) & 3);
        else if constexpr (n < 30) w1p(IC<n - 22>{});
        else w2t(IC<1>{});
      }
      SB;
      aim = MFMA32(fxr[n % 3], fbi[n % 3], aim); pin(aim);
      SB;
      if constexpr (ks == 2) {
        bar<abar(n)>();
        if (n + 2 < 32) rdi(n + 2);
      }
      if (n + 2 < 32) rdx(n + 2);
      if constexpr (ks == 2) {
        if constexpr (n < 22) PIECE(((n - 2) >> 2) + 3, 0);
        else if constexpr (n < 30) w1p(IC<n - 22>{});
        else w2t(IC<0>{});
      }
      SB;
      side(Nn);
    });
    SB;
    STAMP(4);
    hold(4);
    PRIO(2);
    pin(rsv[0], rsv[1], rsv[2], rsv[3]);
    cf u[16], sq[4], pq[4];
#pragma unroll
    for (int i = 0; i < 16; ++i) {
      float a = are[i] * rsv[i >> 2][i & 3], c = aim[i] * rsv[i >> 2][i & 3];
      pin(a, c);
      u[i] = cf{a, c};
    }
    {
#pragma unroll
      for (int k = 1; k < 4; ++k)
#pragma unroll
        for (int q = 0; q < 4; ++q) u[4 * q + k] = cfma(l1, u[4 * q + k - 1], u[4 * q + k]);
      pq[0] = u[3];
#pragma unroll
      for (int q = 1; q < 4; ++q) pq[q] = cfma3(l8, pq[q - 1], u[4 * q + 3]);
      cf T = cmul(lH, pq[3]);
      settle(T);
      LDS(float, aw + (MI_WT - MI_CT)) = add32(T.x, T.y);
    }
    cf t4[4];
    auto meet = [&](auto Oo, int a) AI {
      constexpr int o = decltype(Oo)::v;
      sig<o>(sv);
      _Pragma("nounroll") for (u32 it = 0; it < (SPIN_CAP << 4); ++it) {
        const u32 c = peek<o>(sv);
#pragma unroll
        for (int i = 0; i < 4; ++i) t4[i] = *(volatile LAS f32x2*)(size_t)(a + i * 256);
        if ((u32)__builtin_amdgcn_readfirstlane(c) >= 4u) break;
      }
    };
    meet(IC<C_WT>{}, a8 + (MI_WT - MI_CT));
    cf lin;
    {
      const cf s1 = cfma(l32, t4[0], t4[1]), s2 = cfma(l32, s1, t4[2]), s3 = cfma(l32, s2, t4[3]);
      stg4<0>(u32x4{asu(s3.x), tlo, asu(s3.y), tlo}, cvo, rw, gro);
      PRIO(0);
      lin = csel(rg & 2, csel(rg & 1, s2, s1), csel(rg & 1, t4[0], cf{0.f, 0.f}));
    }
    STAMP(5);
    waitv<5>();
    sig<C_U1>(sv);
#pragma unroll
    for (int q = 0; q < 4; ++q) *(volatile LAS f32x2*)(size_t)(ap + 512 + q * 512) = pq[q];
#pragma unroll
    for (int q = 0; q < 4; ++q) sq[q] = *(volatile LAS f32x2*)(size_t)(aq + q * 512);
    ctile(IC<0>{}); ctile(IC<1>{});
    PRIO(2);
    pin(co2);
#pragma unroll
    for (int i = 0; i < 16; ++i) u[i] = cmul(co2, u[i]);
#pragma unroll
    for (int q = 0; q < 4; ++q) sq[q] = cmul(co2, q ? cfma(l4, pq[q - 1], sq[q]) : sq[q]);
#pragma unroll
    for (int q = 0; q < 4; ++q) pin(u[4 * q], u[4 * q + 1], u[4 * q + 2], u[4 * q + 3], sq[q]);
    pin(lin);
    PRIO(0);
    ctile(IC<2>{}); ctile(IC<3>{});
    w2t(IC<2>{}); w2t(IC<3>{}); w2t(IC<4>{});
    u32x4 lb0, lb1;
    ldg4(lb0, lo0, rw, grs);
    ldg4(lb1, lo1, rw, grs);
    lwait<C_P1, 4>(sv);
    w2t(IC<5>{}); w2t(IC<6>{}); w2t(IC<7>{}); w2t(IC<8>{});
    STAMP(6);
    asm volatile("s_waitcnt vmcnt(8)" : "+v"(lb0), "+v"(lb1)::"memory");
    sig<C_LA>(sv);
    PRIO(2);
    _Pragma("nounroll") for (u32 it = 0; it < SPIN_CAP; ++it) {
      const bool bad = ((d0 >= 0) & (((lb0.y ^ tlo) | (lb0.w ^ tlo)) != 0)) | ((d0 >= 2) & (((lb1.y ^ tlo) | (lb1.w ^ tlo)) != 0));
      if (__builtin_amdgcn_ballot_w64(bad) == 0) break;
      ldg4(lb0, lo0, rw, grs);
      ldg4(lb1, lo1, rw, grs);
      asm volatile("s_waitcnt vmcnt(0)" : "+v"(lb0), "+v"(lb1)::"memory");
    }
    {
      cf a = cmul(lF, cfma(l256, cf{asf(lb1.x), asf(lb1.z)}, cf{asf(lb0.x), asf(lb0.z)}));
      settle(a);
      LDS(float, aw) = add32(a.x, a.y);
    }
    meet(IC<C_HP>{}, a8);
    STAMP(7);
    {
      float zr = (t4[0].x + t4[1].x) + (t4[2].x + t4[3].x);
      pin(zr);
      const cf z = cfma(lL, cf{zr, (t4[0].y + t4[1].y) + (t4[2].y + t4[3].y)}, lin);
#pragma unroll
      for (int q = 0; q < 4; ++q) {
        const cf t = cfma(mq[q], z, sq[q]);
        cf v[4];
#pragma unroll
        for (int k = 0; k < 4; ++k) v[k] = cfma(k == 0 ? l1 : (k == 1 ? l2 : (k == 2 ? l3 : l4)), t, u[4 * q + k]);
        LDS(u32x2, at + 16 * q) = u32x2{pk2(v[0].x, v[1].x), pk2(v[2].x, v[3].x)};
        LDS(u32x2, at + 2304 + 16 * q) = u32x2{pk2(-v[0].y, -v[1].y), pk2(-v[2].y, -v[3].y)};
      }
    }
    sig<C_PAIR>(svp);
    {
      u32x4 f[4];
#pragma unroll
      for (int i = 0; i < 4; ++i) f[i] = BC(u32x4, ldtr(ta + (i >> 1) * 2304 + (i & 1) * 16 * 72, 4 * 72));
      const int so = (int)WS_HS + ((r * 4 + rg) * 32 + s * 2) * (int)REC;
      stg4<0>(f[0], l16, rw, so);
      stg4<1024>(f[1], l16, rw, so);
      stg4<0>(f[2], l16, rw, so + REC);
      stg4<1024>(f[3], l16, rw, so + REC);
      stg1(csum(f[1], csum(f[0], tlo)), lane * 4, rw, so);
      stg1(csum(f[3], csum(f[2], tlo)), lane * 4, rw, so + REC);
    }
    PRIO(0);
    STAMP(8);
    waitv<6>();
    sig<C_LB>(sv);
    lwait<C_P2, 4>(sv);
    ctile(IC<4>{}); ctile(IC<5>{}); ctile(IC<6>{}); ctile(IC<7>{});
    STAMP(9);
    f32x4 ep[4];
    {
      const int col = j * 128 + g * 32 + l31;
      const GAS u16* xp = (const GAS u16*)x + (size_t)(r * 128 + rg * 32 + h * 4) * 512 + col;
      u32 xv[16], dv = ((const GAS u16*)dsk)[col], bv = ((const GAS u16*)b2)[col];
#pragma unroll
      for (int e = 0; e < 16; ++e) xv[e] = xp[((e & 3) + 8 * (e >> 2)) * 512];
      SB;
      waitv<18>();
      sig<C_LC>(sv);
      STAMP(10);
      lwait<C_P3, 4>(sv);
      pin(dv, bv);
      STAMP(11);
      w2t(IC<9>{}); w2t(IC<10>{}); w2t(IC<11>{}); w2t(IC<12>{}); w2t(IC<13>{});
      SB;
#pragma unroll
      for (int e = 0; e < 16; ++e) {
        float xf = bf2f(xv[e]), xn = fmaf(xf, rsv[e >> 2][e & 3], nmv[e >> 2][e & 3]);
        pin(xn);
        xf += bf2f(bv);
        pin(xf);
        xf = fmaf(xn, bf2f(dv), xf);
        pin(xf);
        ep[e >> 2][e & 3] = xf;
      }
    }
#pragma unroll
    for (int q = 0; q < 4; ++q) LDS(f32x4, trb + q * 1024 + l16) = ep[q];
    waitv<0>();
    sig<C_LD>(sv);
    done();
  } else {
    stats();
    f32x16 a0[2] = {}, a1[2];
    bf16x8 xr[32], fw[3][2];
    int wa[2];
    {
      const int aw0 = (8 * h + tq) * 128 + ((tq >> 1) << 6) + (gi & 1) * 32 + tp * 8;
      wa[0] = opq(aw0 + WB);
      wa[1] = opq((aw0 ^ 64) + WB);
    }
    auto rdw = [&](int n, int t) AI {
      if (n == 32) wa[t] = opq((wa[t] + 16384) ^ (j == 1 ? 64 : 0));
      fw[n % 3][t] = ldtr(wa[t] + (n < 32 ? RO[(n >> 2) % 3] + 8192 : (U1S[(n >> 2) & 7] - 8) * 8192) + (n & 3) * 2048, 512);
    };
    auto rdx = [&](int n) AI { xr[n] = ld128(xa[n & 3] + RO[(n >> 2) % 3]); };
    u32x4 rc;
    float sd;
    u32x2 ct[4];
    u32x4 hf[44];
    float gz, gr, go;
    u32 cso[2], hc[22], cv, c1, c2, c3;
    auto gelu = [&](const f32x16* a, int t, int o) AI {
      const int v = t >> 1;
      if (!(t & 1)) {
        if (v < 32) {
          const float x_ = a[v >> 4][v & 15];
          gz = __builtin_amdgcn_exp2f(fmaf(asf(rc.y), x_ * x_, asf(rc.x)) * x_);
          pin(gz);
        }
      } else {
        if (v < 32) gz = fmaf(gz, sd, sd);
        if (v > 0) {
          const float y = a[(v - 1) >> 4][(v - 1) & 15] * gr;
          if (v & 1) {
            go = y;
            pin(go);
          } else {
            const u32 p = pk2(go, y);
            const int i = (v - 2) >> 1, f = i >> 2;
            hf[o ? (f < 2 ? KT + f : 40 + f) : f][i & 3] = p;
            if ((i & 7) == 0) cso[i >> 3] = tlo;
            cso[i >> 3] = (p << (i & 3)) + cso[i >> 3];
            pin(cso[i >> 3]);
          }
        }
        if (v < 32) {
          gr = __builtin_amdgcn_rcpf(gz);
          pin(gr);
        }
      }
    };
    const int hq = (int)WS_HF + ((r * 4 + rg) * 56 + g * 14) * (int)REC, hso = (int)WS_HS + (r * 4 + rg) * 32 * (int)REC;
    const int l4 = opq(lane * 4);
    SB;
    bar<4>();
    STAMP(3);
    hold(3);
    rdw(0, 1); rdw(0, 0); rdx(0); rdw(1, 1); rdw(1, 0); rdx(1);
    sfor<0, 32>([&](auto Nn) AI {
      constexpr int n = decltype(Nn)::v, ks = n & 3;
      SB;
      a0[0] = MFMA32(fw[n % 3][0], xr[n], a0[0]); pin(a0[0]);
      SB;
      if (ks != 2) {
        rdw(n + 2, 1);
        rdw(n + 2, 0);
      }
      if constexpr (n < 2) PIECE(2, 2 * n);
      SB;
      a0[1] = MFMA32(fw[n % 3][1], xr[n], a0[1]); pin(a0[1]);
      SB;
      if (ks == 2) {
        bar<4>();
        STS(4, 5 + (n >> 2));
        if (n == 30) {
          rc = LDS(u32x4, MI_RC + (rg * 32 + l31) * 16);
          const int a = opq(MI_CT + l31 * 8);
#pragma unroll
          for (int t = 0; t < 4; ++t) ct[t] = LDS(u32x2, a + t * 256);
        }
        rdw(n + 2, 1);
        rdw(n + 2, 0);
      }
      if (n + 2 < 32) rdx(n + 2);
      if constexpr (n < 2) PIECE(2, 2 * n + 1);
      else if constexpr (n < 22) PIECE(((n - 2) >> 2) + 3, (n - 2) & 3);
      else if constexpr (n < 30) w1p(IC<n - 22>{});
    });
    SB;
    PRIO(1);
    {
      const bf16x8 cb = BC(bf16x8, (u32x4{h ? 0u : rc.z, h ? 0u : rc.w, 0u, 0u}));
      sd = bf2f(rc.z);
      a0[0] = MFMA32(BC(bf16x8, (u32x4{ct[0].x, ct[0].y, ct[1].x, ct[1].y})), cb, a0[0]); pin(a0[0]);
      a1[0] = MFMA32(BC(bf16x8, (u32x4{ct[2].x, ct[2].y, ct[3].x, ct[3].y})), cb, f32x16{}); pin(a1[0]);
      const bf16x8 cc = BC(bf16x8, (u32x4{0u, 0u, h ? 0u : rc.z, h ? 0u : rc.w}));
      a0[1] = MFMA32(BC(bf16x8, (u32x4{ct[0].x, ct[0].y, ct[1].x, ct[1].y})), cc, a0[1]); pin(a0[1]);
      a1[1] = MFMA32(BC(bf16x8, (u32x4{ct[2].x, ct[2].y, ct[3].x, ct[3].y})), cc, f32x16{}); pin(a1[1]);
    }
    STAMP(4);
    hold(4);
    f32x16 acc[4] = {};
    bf16x8 wf[2][4];
    u32 pk = 0;
    int a2[2][4], ac[4], ta;
    auto rd2 = [&](int k, int i, int buf) AI {
      if ((k >= KS && k < KS + 4) || (k >= 22 && k < 34)) {
        const int part = k < 10 ? (k - KS) >> 1 : (k < 26 ? (k - 22) >> 1 : 2 + ((k - 26) >> 2));
        const int q = k < 10 ? k & 1 : (k < 26 ? 2 + (k & 1) : (k - 26) & 3);
        wf[buf][i] = ld128(ac[q] + part * 16384 + i * 4096);
      } else {
        const int p = k < 4 ? k >> 1 : (k < 10 ? 2 : (k < 22 ? (k - 4) >> 1 : (k - 16) >> 1));
        const int sl = W2S[p];
        wf[buf][i] = ldtr(a2[sl >= 12][i] + (sl >= 12 ? sl - 12 : sl) * 8192 + (k & 1) * 4096, 2048);
      }
    };
    auto rec = [&](int k) AI {
      if (k >= 22 && k < 34) {
        const int d = k < 26 ? 1 : 2 + (((k - 26) >> 1) & 1), part = k < 26 ? (k - 22) >> 1 : (k - 26) >> 2;
        return hso + ((opqs(s) ^ d) * 2 + part) * (int)REC;
      }
      return hq + tile(k < 4 ? k >> 1 : (k < 10 ? 2 : (k < 22 ? (k - 4) >> 1 : (k - 16) >> 1))) * (int)REC;
    };
    int sso, lso[22];
    auto event = [&](auto Ee) AI {
      constexpr int idx = decltype(Ee)::v, kind = EV.e[idx].kind, t = EV.e[idx].arg / 3, a = EV.e[idx].arg % 3;
      if constexpr (kind == E_ST) {
        constexpr int k = t < 2 ? 2 * t : (t == 2 ? KT : 42);
        const int off = (t == 3 && j < 2) ? OOB : 0;
        if constexpr (a == 0) sso = rec(k);
        if constexpr (a < 2) stg4<a * 1024>(hf[k + a], l16 | off, rw, sso);
        else stg1(cso[t & 1], l4 | off, rw, sso);
      } else {
        if constexpr (a == 0) lso[t] = rec(2 * t);
        if constexpr (a < 2) ldg4o<a * 1024>(hf[2 * t + a], l16, rw, lso[t]);
        else ldg1(hc[t], l4, rw, lso[t]);
      }
    };
    auto fill = [&](auto Gg) AI {
      constexpr int G = decltype(Gg)::v;
#pragma unroll
      for (int t = gsub(G, GA0, GA1); t < gsub(G + 1, GA0, GA1); ++t) gelu(a0, t, 0);
#pragma unroll
      for (int t = gsub(G, GB0, GB1); t < gsub(G + 1, GB0, GB1); ++t) gelu(a1, t, 1);
      sfor<evfirst(G), evfirst(G + 1)>(event);
    };
    sfor<32, 64>([&](auto Nn) AI {
      constexpr int n = decltype(Nn)::v, m = n - 32;
      SB;
      a1[0] = MFMA32(fw[n % 3][0], xr[m], a1[0]); pin(a1[0]);
      SB;
      if (m == 14) chk<C_U1, 8>(sv, pk);
      if (n + 2 < 64) rdw(n + 2, 1);
      if (m == 30) chk<C_LA, 4>(sv, pk);
      if (m >= 30) rd2(0, 2 * (m - 30), 0);
      fill(IC<2 * m>{});
      SB;
      a1[1] = MFMA32(fw[n % 3][1], xr[m], a1[1]); pin(a1[1]);
      SB;
      if (n + 2 < 64) rdw(n + 2, 0);
      if (m >= 30) rd2(0, 2 * (m - 30) + 1, 0);
      if (m == 3) {
        waitv<0>();
        sig<C_U1>(sv);
      }
      fill(IC<2 * m + 1>{});
      if (m == 13) pk = peek<C_U1>(sv);
      if (m == 15) sig<C_P1>(sv);
      if (m == 31) sig<C_P2>(sv);
      if (m == 29) pk = peek<C_LA>(sv);
      if (m == 28) {
        const int a = (4 * h + tq) * 256 + (tq << 6) + (gi & 1) * 32 + tp * 8;
#pragma unroll
        for (int i = 0; i < 4; ++i) a2[0][i] = opq(a ^ (i << 6));
      }
      if (m == 29) {
#pragma unroll
        for (int i = 0; i < 4; ++i) a2[1][i] = opq(a2[0][i] + 98304);
      }
      if (m == 30) {
#pragma unroll
        for (int i = 0; i < 4; ++i) ac[i] = opq(ax ^ (i * 32 + 32768) ^ ((j & 1) * 64));
      }
      if (m == 31) ta = opq(trb + (8 * h + tq) * 72 + (16 * (gi & 1) + 4 * tp) * 2);
      if (m == 7) STS(1, 5);
      if (m == 15) STS(1, 6);
      if (m == 23) STS(1, 7);
    });
    STS(0, 5); STS(1, 8);
    sfor<0, 44>([&](auto Kk) AI {
      constexpr int k = decltype(Kk)::v;
      sfor<(k >= KD ? 1 : 0), 4>([&](auto Ii) AI {
        constexpr int i = decltype(Ii)::v;
        SB;
        acc[i] = MFMA32(BC(bf16x8, hf[k]), wf[k & 1][i], acc[i]); pin(acc[i]);
        SB;
        if (i == 1) {
          if (k == 12) chk<C_LB, 4>(sv, pk);
          if (k == 24) chk<C_LC, 4>(sv, pk);
          if (k == 32) chk<C_LD, 4>(sv, pk);
        }
        if (k + 1 < 44 && (i > 0 || k + 1 < KD)) rd2(k + 1, i, (k + 1) & 1);
        if constexpr (k == 43 && i > 1) {
          rd2(KD + i - 2, 0, 0);
          wf[0][i - 1] = wf[0][0];
        }
        if (k == KS - 1 && i == 1) {
          chk<C_PAIR, 1>(svp, pk);
#pragma unroll
          for (int f = 0; f < 4; ++f) hf[KS + f] = BC(u32x4, ldtr(ta + (f >> 1) * 2304 + (f & 1) * 16 * 72, 4 * 72));
        }
        if constexpr (k >= 9 && k < 43 && (k & 1)) {
          if constexpr (i == 1) {
            asm volatile("s_waitcnt vmcnt(%3)" : "+v"(hf[k + 1]), "+v"(hf[k + 2]), "+v"(hc[(k + 1) / 2])
                         : "n"(evfirst(gapof(k, 1)) - evfind(E_LD, (k + 1) / 2 * 3 + 2) - 1) : "memory");
            c1 = (hf[k + 1].y + hf[k + 2].y) << 1;
            c2 = (hf[k + 1].z + hf[k + 2].z) << 2;
            c3 = (hf[k + 1].w + hf[k + 2].w) << 3;
            pin(c1, c2, c3);
          }
          if constexpr (i == 2) {
            cv = (hf[k + 1].x + hf[k + 2].x + tlo) + (c1 + c2 + c3);
            pin(cv);
          }
          if constexpr (i == 3) {
            if (__builtin_expect(__builtin_amdgcn_ballot_w64(cv != hc[(k + 1) / 2]) != 0, 0)) {
              RETRY(k);
              refetch(hf[k + 1], hf[k + 2], l16, rw, rec(k + 1), tlo);
            }
          }
        }
        fill(IC<gapof(k, i)>{});
        if (k == KS - 1 && i == 0) pk = peek<C_PAIR>(svp);
        if (i == 3) {
          if (k == 11) pk = peek<C_LB>(sv);
          if (k == 23) pk = peek<C_LC>(sv);
          if (k == 31) pk = peek<C_LD>(sv);
          if (k == 13) sig<C_P3>(sv);
        }
      });
      if (k == 3) STS(1, 9);
      if (k == 5) STS(1, 10);
      if (k == 7) STS(1, 11);
      if (k == 9) STS(1, 12);
      if (k == 10) STS(1, 13);
      if (k == 12) STS(1, 14);
      if (k == 9) STS(0, 6);
      if (k == 15) STS(0, 7);
      if (k == 21) STS(0, 8);
      if (k == 27) STS(0, 9);
      if (k == 33) STS(0, 10);
      if (k == 39) STS(0, 11);
    });
    STS(0, 12);
    u32x4 xo;
    sfor<KD, 44>([&](auto Kk) AI {
      constexpr int k = decltype(Kk)::v, t = k - KD;
      SB;
      acc[0] = MFMA32(BC(bf16x8, hf[k]), wf[0][1 + t % 3], acc[0]); pin(acc[0]);
      SB;
      if (k + 2 < 44) {
        rd2(k + 2, 0, 0);
        wf[0][1 + (t + 2) % 3] = wf[0][0];
      }
      if constexpr (t < 12) {
        constexpr int i = 1 + (t >> 2), e = (t & 3) * 4;
        u32 p0 = pk2(acc[i][e], acc[i][e + 1]), p1 = pk2(acc[i][e + 2], acc[i][e + 3]);
        if ((t & 3) == 0) cv = tlo;
        cv = (p1 << ((t & 1) * 2 + 1)) + ((p0 << ((t & 1) * 2)) + cv);
        pin(p0, p1, cv);
        xo[(t & 1) * 2] = p0;
        xo[(t & 1) * 2 + 1] = p1;
        const int so = (int)WS_X + ((((r * 4 + j) * 4 + (g ^ i)) * 4 + rg) * 3 + i - 1) * (int)REC;
        if (t & 1) stg4<((t >> 1) & 1) * 1024>(xo, l16, rw, so);
        if ((t & 3) == 3) stg1(cv, l4, rw, so);
      }
    });
    SB;
    STS(0, 13);
    const int xin = (int)WS_X + (((r * 4 + j) * 4 + g) * 4 + rg) * 3 * (int)REC;
    u32x4 xi[6];
    u32 xc[3];
    f32x4 ep[4];
#pragma unroll
    for (int q = 0; q < 4; ++q) ep[q] = LDS(f32x4, trb + q * 1024 + l16);
#pragma unroll
    for (int i = 0; i < 3; ++i) {
      waitv<6>();
      ldg4o<0>(xi[2 * i], l16, rw, xin + i * REC);
      ldg4o<1024>(xi[2 * i + 1], l16, rw, xin + i * REC);
      ldg1(xc[i], l4, rw, xin + i * REC);
    }
    f32x2 o[8];
#pragma unroll
    for (int e = 0; e < 8; ++e) o[e] = f32x2{acc[0][2 * e], acc[0][2 * e + 1]} + f32x2{ep[e >> 1][(2 * e) & 3], ep[e >> 1][(2 * e + 1) & 3]};
    sfor<0, 3>([&](auto Ii) AI {
      constexpr int i = decltype(Ii)::v;
      asm volatile("s_waitcnt vmcnt(%3)" : "+v"(xi[2 * i]), "+v"(xi[2 * i + 1]), "+v"(xc[i]) : "n"(6 - 3 * i) : "memory");
      if (__builtin_amdgcn_ballot_w64(csum(xi[2 * i + 1], csum(xi[2 * i], tlo)) != xc[i]) != 0) {
        RETRY(50 + 2 * i);
        refetch(xi[2 * i], xi[2 * i + 1], l16, rw, xin + i * REC, tlo);
      }
#pragma unroll
      for (int e = 0; e < 8; ++e) {
        const u32 u = xi[2 * i + (e >> 2)][e & 3];
        o[e] += f32x2{bf2f(u), bfh(u)};
      }
#pragma unroll
      for (int e = 0; e < 8; ++e) pin(o[e]);
    });
    STS(0, 14);
    GAS float* op = (GAS float*)out + (size_t)(r * 128 + rg * 32 + h * 4) * 512 + j * 128 + g * 32 + l31;
#pragma unroll
    for (int e = 0; e < 16; ++e) __hip_atomic_store(&op[((e & 3) + 8 * (e >> 2)) * 512], (float)o[e >> 1][e & 1], __ATOMIC_RELAXED, __HIP_MEMORY_SCOPE_AGENT);
    if (r == 15 && s == 0 && w == 4) stg2<0>(u32x2{tlo, tlo}, lane == 0 ? 0 : OOB, rw);
    done();
  }
}

__global__ __launch_bounds__(512, 2) void fused_ssm_ffn(const u16* __restrict__ x, const u16* __restrict__ w1, const u16* __restrict__ bre,
                                                         const u16* __restrict__ bim, char* __restrict__ ws, const u16* __restrict__ b1,
                                                         const u16* __restrict__ lre, const u16* __restrict__ lim, const u16* __restrict__ ldt,
                                                         const u16* __restrict__ cre, const u16* __restrict__ cim, const u16* __restrict__ w2,
                                                         const u16* __restrict__ dsk, const u16* __restrict__ b2, float* __restrict__ out) {
  u64 stp[1] = {0};
  STAMP(0);
  const u64 ka = (u64)__builtin_amdgcn_kernarg_segment_ptr();
  u32 j0, j1;
  asm volatile("s_load_dword %0, %2, 0\n\ts_load_dword %1, %2, 64" : "=&s"(j0), "=&s"(j1) : "s"(ka) : "memory");
  const int w = __builtin_amdgcn_readfirstlane(threadIdx.x >> 6);
  if (w < 4) role<true>(w, threadIdx.x, ka, j0, j1, stp[0]);
  else role<false>(w, threadIdx.x, ka, j0, j1, stp[0]);
}

extern "C" void kernel_launch(void* const* d_in, const int* in_sizes, int n_in, void* d_out, int out_size, void* d_ws, size_t ws_size,
                              hipStream_t stream) {
  (void)hipFuncSetAttribute((const void*)fused_ssm_ffn, hipFuncAttributeMaxDynamicSharedMemorySize, LDS_BYTES);
  fused_ssm_ffn<<<256, 512, LDS_BYTES, stream>>>((const u16*)d_in[0], (const u16*)d_in[10], (const u16*)d_in[3], (const u16*)d_in[4], (char*)d_ws,
                                                 (const u16*)d_in[11], (const u16*)d_in[1], (const u16*)d_in[2], (const u16*)d_in[7],
                                                 (const u16*)d_in[5], (const u16*)d_in[6], (const u16*)d_in[12], (const u16*)d_in[8],
                                                 (const u16*)d_in[13], (float*)d_out);
}
```

```cpp
#include <hip/hip_runtime.h>
#include <hip/hip_bf16.h>

typedef unsigned short u16;
typedef unsigned u32;
typedef unsigned long long u64;
typedef short bf16x8 __attribute__((ext_vector_type(8)));
typedef short bf16x4 __attribute__((ext_vector_type(4)));
typedef float f32x16 __attribute__((ext_vector_type(16)));
typedef float f32x4 __attribute__((ext_vector_type(4)));
typedef float f32x2 __attribute__((ext_vector_type(2)));
typedef __bf16 bf16x2n __attribute__((ext_vector_type(2)));
typedef u32 u32x4 __attribute__((ext_vector_type(4)));
typedef u32 u32x2 __attribute__((ext_vector_type(2)));
typedef int i32x4 __attribute__((ext_vector_type(4)));
#define LAS __attribute__((address_space(3)))
#define DI __device__ __forceinline__
#define AI __attribute__((always_inline))
#define SB __builtin_amdgcn_sched_barrier(0)
#define PRIO(n) do { SB; asm volatile("s_setprio " #n); SB; } while (0)
#define MFMA32(a, b, c) __builtin_amdgcn_mfma_f32_32x32x16_bf16((a), (b), (c), 0, 0, 0)
#define BC(T, v) __builtin_bit_cast(T, v)

constexpr u32 REC = 2304;
constexpr u32 WS_GR = 4096;
constexpr u32 WS_GS = WS_GR + 131072;
constexpr u32 WS_GB = WS_GS + 16384;
constexpr u32 WS_GW = WS_GB + 8192;
constexpr u32 WS_HF = 524288;
constexpr u32 WS_HS = WS_HF + 64 * 56 * REC;
constexpr u32 WS_X = WS_HS + 64 * 32 * REC;
constexpr u32 WS_END = WS_X + 1024 * 3 * REC;
static_assert(WS_GW + 8 * 1792 * 8 <= WS_HF, "ws map");

constexpr int WB = 49152;
constexpr int RO[3] = {16384, 0, 32768};
constexpr int U1S[8] = {12, 13, 14, 15, 10, 11, 8, 9};
constexpr int W2S[14] = {16, 0, 1, 2, 3, 12, 13, 14, 15, 16, 0, 1, 2, 3};
constexpr int MI_TR = 139264, MI_PW = MI_TR, MI = MI_TR + 8 * 2304;
constexpr int MI_PS = MI, MI_CT = MI + 128, MI_RC = MI_CT + 1024, MI_RS = MI_RC + 2048, MI_NM = MI_RS + 512, MI_WT = MI_NM + 512, MI_HP = MI_WT + 1024,
              MI_CNT = MI_HP, MI_JUNK = MI_CNT + 128;
constexpr int MI_LT = MI_TR + 5120;
constexpr int MI_HQ = MI_CT;
constexpr int LDS_BYTES = 163840;
static_assert(MI_JUNK + 256 + 128 <= LDS_BYTES, "lds map");
constexpr int C_WT = 0, C_HP = 4, C_U1 = 8, C_LA = 12, C_LB = 16, C_LC = 20, C_LD = 24, C_P1 = 28, C_P2 = 32, C_P3 = 36, C_P4 = 40, C_LE = 44, C_P5 = 48, C_PAIR = 64;
constexpr int OOB = 0x40000000;
#define CAPB 18
constexpr u32 SPIN_CAP = 1u << CAPB;

#define TIMER "s_memrealtime"
#define STAMP(i)
#define SET 0
#define RETRY(k)
#define STS(set, i) do { if (SET == (set)) STAMP(i); } while (0)
#define STA(i) do { if (SET != 5 && SET != 9) STAMP(i); } while (0)
#define STN(i)
#define STF(i)

DI float asf(u32 b) { return BC(float, b); }
DI u32 asu(float f) { return BC(u32, f); }
DI float bf2f(u32 b) { return asf(b << 16); }
DI float bfh(u32 b) { return asf(b & 0xffff0000u); }
DI u32 pk2(float a, float b) {
  f32x2 v = {a, b};
  return BC(u32, __builtin_convertvector(v, bf16x2n));
}
DI float rbf(float a) { return bf2f(pk2(a, a)); }
template <int SI, int LI> DI void dma1(i32x4 rs, int v, int sb, int lw) {
  int t;
  asm volatile("s_add_i32 m0, %[lw], %[li]\n\ts_add_i32 %[t], %[sb], %[si]\n\tbuffer_load_dwordx4 %[v], %[rs], %[t] offen lds"
               : [t] "=&s"(t)
               : [v] "v"(v), [rs] "s"(rs), [sb] "s"(sb), [lw] "s"(lw), [si] "n"(SI), [li] "n"(LI)
               : "memory", "scc");
}
template <int LI> DI void dma_s(i32x4 rs, int v, int& so, int ps, int lw) {
  asm volatile("s_add_i32 m0, %[lw], %[li]\n\ts_add_i32 %[t], %[t], %[ps]\n\tbuffer_load_dwordx4 %[v], %[rs], %[t] offen lds"
               : [t] "+s"(so)
               : [v] "v"(v), [rs] "s"(rs), [ps] "s"(ps), [lw] "s"(lw), [li] "n"(LI)
               : "memory", "scc");
}
template <int O> DI void dma_n(i32x4 rs, int v, int so) {
  asm volatile("buffer_load_dwordx4 %0, %1, %2 offen offset:%3 lds" ::"v"(v), "s"(rs), "s"(so), "n"(O) : "memory");
}
template <int LI> DI void dma2(i32x4 rs, int v0, int v1, int so, int lw) {
  asm volatile("s_add_i32 m0, %4, %5\n\ts_nop 0\n\tbuffer_load_dwordx4 %0, %2, %3 offen lds\n\tbuffer_load_dwordx4 %1, %2, %3 offen offset:1024 lds"
               :
               : "v"(v0), "v"(v1), "s"(rs), "s"(so), "s"(lw), "n"(LI)
               : "memory", "scc");
}
template <int I> struct IC { static constexpr int v = I; };
template <int A, int B, class Fn> DI void sfor(Fn&& f) {
  if constexpr (A < B) {
    f(IC<A>{});
    sfor<A + 1, B>(f);
  }
}
typedef f32x2 cf;
template <class T> DI void pin1(T& v) { asm volatile("" : "+v"(v)); }
template <class... T> DI void pin(T&... v) { (pin1(v), ...); }
DI int opq(int v) { asm volatile("" : "+v"(v)); return v; }
DI int opqs(int v) { asm volatile("" : "+s"(v)); return v; }
template <int N> DI void bar() { asm volatile("s_waitcnt vmcnt(%0) lgkmcnt(0)\n\ts_barrier" ::"n"(N) : "memory"); }
template <int N> DI void waitv() { asm volatile("s_waitcnt vmcnt(%0)" ::"n"(N) : "memory"); }
DI void waitl() { asm volatile("s_waitcnt lgkmcnt(0)" ::: "memory"); }
DI bf16x8 ld128(int a) { return *(LAS bf16x8*)(size_t)a; }
DI bf16x8 ldtr(int a, int d) {
  bf16x4 lo = __builtin_amdgcn_ds_read_tr16_b64_v4i16((LAS bf16x4*)(size_t)a);
  bf16x4 hi = __builtin_amdgcn_ds_read_tr16_b64_v4i16((LAS bf16x4*)(size_t)(a + d));
  return __builtin_shufflevector(lo, hi, 0, 1, 2, 3, 4, 5, 6, 7);
}
#define LDS(T, a) (*(LAS T*)(size_t)(a))
DI float add32(float a, float b) {
  auto r = __builtin_amdgcn_permlane32_swap(asu(a), asu(b), false, false);
  return asf(r[0]) + asf(r[1]);
}
DI float add16(float a, float b) {
  auto r = __builtin_amdgcn_permlane16_swap(asu(a), asu(b), false, false);
  return asf(r[0]) + asf(r[1]);
}
template <int C> DI float dppadd(float v) { return v + asf((u32)__builtin_amdgcn_update_dpp(0, (int)asu(v), C, 0xf, 0xf, true)); }
DI void halves(float v, float& a, float& b) {
  auto r = __builtin_amdgcn_permlane32_swap(asu(v), asu(v), false, false);
  a = asf(r[0]);
  b = asf(r[1]);
}
DI u32 split2(float c) {
  return pk2(c, c - rbf(c));
}
DI cf cfma(cf a, cf b, cf c) {
  float x = c.x, y = c.y;
  asm("v_fmac_f32 %0, %2, %4\n\tv_fmac_f32 %1, %2, %5\n\tv_fma_f32 %0, -%3, %5, %0\n\tv_fmac_f32 %1, %3, %4"
      : "+&v"(x), "+&v"(y)
      : "v"(a.x), "v"(a.y), "v"(b.x), "v"(b.y));
  return cf{x, y};
}
DI cf cfma3(cf a, cf b, cf c) {
  float x, y;
  asm("v_fma_f32 %0, %2, %4, %6\n\tv_fma_f32 %1, %2, %5, %7\n\tv_fma_f32 %0, -%3, %5, %0\n\tv_fmac_f32 %1, %3, %4"
      : "=&v"(x), "=&v"(y)
      : "v"(a.x), "v"(a.y), "v"(b.x), "v"(b.y), "v"(c.x), "v"(c.y));
  return cf{x, y};
}
DI cf cmul(cf a, cf b) {
  float x, y;
  asm("v_mul_f32 %0, %2, %4\n\tv_mul_f32 %1, %2, %5\n\tv_fma_f32 %0, -%3, %5, %0\n\tv_fmac_f32 %1, %3, %4"
      : "=&v"(x), "=&v"(y)
      : "v"(a.x), "v"(a.y), "v"(b.x), "v"(b.y));
  return cf{x, y};
}
DI void fin2(cf a, cf b, cf t, cf u, cf v, u32& re, u32& im) {
  float x, y;
  asm("v_fma_f32 %0, %4, %8, %10\n\tv_fma_f32 %1, %4, %9, %11\n\tv_fma_f32 %2, %6, %8, %12\n\tv_fma_f32 %3, %6, %9, %13\n\t"
      "v_fma_f32 %0, -%5, %9, %0\n\tv_fma_f32 %2, -%7, %9, %2\n\tv_fmac_f32 %1, %5, %8\n\tv_fmac_f32 %3, %7, %8\n\t"
      "v_cvt_pk_bf16_f32 %0, %0, %2\n\tv_cvt_pk_bf16_f32 %1, -%1, -%3"
      : "=&v"(re), "=&v"(im), "=&v"(x), "=&v"(y)
      : "v"(a.x), "v"(a.y), "v"(b.x), "v"(b.y), "v"(t.x), "v"(t.y), "v"(u.x), "v"(u.y), "v"(v.x), "v"(v.y));
}
DI cf csel(bool c, cf a, cf b) { return cf{c ? a.x : b.x, c ? a.y : b.y}; }
DI void settle(cf& a) { asm volatile("s_nop 1" : "+v"(a)); }
template <int O> DI void sig(int sv) { asm volatile("ds_inc_u32 %0, %0 offset:%1" ::"v"(sv), "n"(O) : "memory"); }
template <int O> DI u32 peek(int sv) { return *(volatile LAS u32*)(size_t)(sv + O); }
template <int O, int N> DI void chk(int sv, u32 v) {
  int t, c;
  asm volatile("v_readfirstlane_b32 %[t], %[v]\n\ts_cmp_ge_u32 %[t], %[n]\n\ts_cbranch_scc1 2f\n\ts_mov_b32 %[c], 0x400000\n"
               "1:\n\ts_sleep 1\n\tds_read_b32 %[v], %[a] offset:%[o]\n\ts_sub_u32 %[c], %[c], 1\n\ts_waitcnt lgkmcnt(0)\n\ts_cbranch_scc1 2f\n\t"
               "v_readfirstlane_b32 %[t], %[v]\n\ts_cmp_lt_u32 %[t], %[n]\n\ts_cbranch_scc1 1b\n2:"
               : [t] "=&s"(t), [c] "=&s"(c), [v] "+v"(v)
               : [a] "v"(sv), [o] "n"(O), [n] "n"(N)
               : "memory", "scc");
}
template <int O, int N> DI void lwait(int sv) { chk<O, N>(sv, peek<O>(sv)); }
template <int O, int N> DI void chkv(int sv, u32 v) {
  if (__builtin_expect(__builtin_amdgcn_ballot_w64(v < (u32)N) != 0, 0)) lwait<O, N>(sv);
}
DI void ldg4(u32x4& d, int vo, i32x4 rs, int so) {
  asm volatile("buffer_load_dwordx4 %0, %1, %2, %3 offen sc1" : "=v"(d) : "v"(vo), "s"(rs), "s"(so) : "memory");
}
template <int O> DI void ldg4o(u32x4& d, int vo, i32x4 rs, int so) {
  asm volatile("buffer_load_dwordx4 %0, %1, %2, %3 offen offset:%4 sc1" : "=v"(d) : "v"(vo), "s"(rs), "s"(so), "n"(O) : "memory");
}
DI void ldg1(u32& d, int vo, i32x4 rs, int so) {
  asm volatile("buffer_load_dword %0, %1, %2, %3 offen offset:2048 sc1" : "=v"(d) : "v"(vo), "s"(rs), "s"(so) : "memory");
}
DI void stg1(u32 d, int vo, i32x4 rs, int so) {
  asm volatile("buffer_store_dword %0, %1, %2, %3 offen offset:2048 sc1" ::"v"(d), "v"(vo), "s"(rs), "s"(so) : "memory");
}
template <int N> DI void wait3(u32x4& a, u32x4& b, u32& c) { asm volatile("s_waitcnt vmcnt(%3)" : "+v"(a), "+v"(b), "+v"(c) : "n"(N) : "memory"); }
DI u32 add3(u32 a, u32 b, u32 c) { return (a + b) + c; }
DI u32 csum(u32x4 a, u32x4 b, u32 tag) { return add3(add3(add3(a.x, a.y, tag), a.z, a.w), add3(b.x, b.y, b.z), b.w); }
DI u32 csum(u32x4 a, u32x4 b, u32x4 c, u32x4 d, u32 tag) { return add3(add3(csum(a, b, tag), c.x, c.y), add3(add3(c.z, c.w, d.x), d.y, d.z), d.w); }
DI void refetch2(u32x4* f, int l16, i32x4 rs, int so, u32 tag) {
  _Pragma("nounroll") for (u32 it = 0; it < SPIN_CAP; ++it) {
    u32 c;
    asm volatile("buffer_load_dwordx4 %0, %5, %7, %8 offen sc1\n\tbuffer_load_dwordx4 %1, %5, %7, %8 offen offset:1024 sc1\n\t"
                 "buffer_load_dwordx4 %2, %5, %7, %8 offen offset:2304 sc1\n\tbuffer_load_dwordx4 %3, %5, %7, %8 offen offset:3328 sc1\n\t"
                 "buffer_load_dword %4, %6, %7, %8 offen offset:2048 sc1\n\ts_waitcnt vmcnt(0)"
                 : "=&v"(f[0]), "=&v"(f[1]), "=&v"(f[2]), "=&v"(f[3]), "=&v"(c)
                 : "v"(l16), "v"(l16 >> 2), "s"(rs), "s"(so)
                 : "memory");
    if (__builtin_amdgcn_ballot_w64(csum(f[0], f[1], f[2], f[3], tag) != c) == 0) break;
  }
}
DI void refetch(u32x4& a, u32x4& b, int l16, i32x4 rs, int so, u32 tag) {
  _Pragma("nounroll") for (u32 it = 0; it < SPIN_CAP; ++it) {
    u32 c;
    asm volatile("buffer_load_dwordx4 %0, %3, %5, %6 offen sc1\n\tbuffer_load_dwordx4 %1, %3, %5, %6 offen offset:1024 sc1\n\t"
                 "buffer_load_dword %2, %4, %5, %6 offen offset:2048 sc1\n\ts_waitcnt vmcnt(0)"
                 : "=&v"(a), "=&v"(b), "=&v"(c)
                 : "v"(l16), "v"(l16 >> 2), "s"(rs), "s"(so)
                 : "memory");
    if (__builtin_amdgcn_ballot_w64(csum(a, b, tag) != c) == 0) break;
  }
}
template <int O> DI void stg4(u32x4 d, int vo, i32x4 rs, int so) {
  asm volatile("buffer_store_dwordx4 %0, %1, %2, %3 offen offset:%4 sc1" ::"v"(d), "v"(vo), "s"(rs), "s"(so), "n"(O) : "memory");
}
template <int O> DI void stf(float d, int vo, i32x4 rs, int so) {
  asm volatile("buffer_store_dword %0, %1, %2, %3 offen offset:%4 sc1" ::"v"(d), "v"(vo), "s"(rs), "s"(so), "n"(O) : "memory");
}
DI void lanes3(int& v, int a, int b, int c) {
  int t;
  asm volatile("v_add_u32 %0, %2, %0\n\ts_add_u32 %1, %3, 8\n\tv_writelane_b32 %0, %3, 16\n\tv_writelane_b32 %0, %1, 17\n\tv_writelane_b32 %0, %4, 18"
               : "+v"(v), "=&s"(t)
               : "s"(a), "s"(b), "s"(c)
               : "scc");
}
DI void touch(int& v, u64 p) { asm volatile("global_load_dword %0, %0, %1" : "+v"(v) : "s"(p) : "memory"); }
template <int C> DI void xread(u32x2* xv, int a, int c) {
  int t;
  asm volatile("s_cmp_lg_u32 %6, %7\n\ts_cbranch_scc1 1f\n\tv_xor_b32 %4, 64, %5\n\tds_read_b64_tr_b16 %0, %5\n\t"
               "ds_read_b64_tr_b16 %2, %5 offset:2048\n\tds_read_b64_tr_b16 %1, %4 offset:1024\n\tds_read_b64_tr_b16 %3, %4 offset:3072\n1:"
               : "+v"(xv[0]), "+v"(xv[1]), "+v"(xv[2]), "+v"(xv[3]), "=&v"(t)
               : "v"(a), "s"(c), "n"(C)
               : "memory", "scc");
}
template <int O> DI void stg2(u32x2 d, int vo, i32x4 rs) {
  asm volatile("buffer_store_dwordx2 %0, %1, %2, 0 offen offset:%3 sc1" ::"v"(d), "v"(vo), "s"(rs), "n"(O) : "memory");
}

#define QP 3
#define QG 34
#define QC 46
constexpr int S1 = 1, S2 = 2, S3 = 4, S4 = 5, S5 = 8, S6 = 9, S7 = 10, S8 = 11;
#define QT 47
#define QL 0
#define BD3 8
constexpr int aops(int q) { return ((q & 1) || q < 4) + (q == 61 || q == 63) + (q == QP) + (q == QT) + 4 * (q == QG); }
constexpr int asince(int q0, int q1) { int c = 0; for (int q = q0; q < q1; ++q) c += aops(q); return c; }
constexpr int abar(int nb) { return nb < 6 ? asince(0, 2 * nb) : asince(2 * nb - 9, 2 * nb) - 1; }
constexpr int bdma(int q) {
  for (int i = 0; i < 28; ++i)
    if (q == 8 * (i >> 2) + 4 + ((i & 3) == 3 ? BD3 : 2 + 2 * (i & 3))) return i;
  return -1;
}
#define KS 6
#define KT 4
#define GA0 0
#define GA1 56
#define GB0 66
#define GB1 100
#define KD0 14
#define KD3 28
#define KD2 38
#define PC 13
#define XL1 6
#define XL2 3
#define PD 5
constexpr int WR = PD + 2, GE = 64 + 176;
constexpr int gk(int u) { return u < 7 ? 10 + 4 * u : 40; }
constexpr int gn(int u) { return u == 6 ? 6 : 4; }
constexpr int gl(int u) { return u < 6 ? 5 : (u == 6 ? 9 : 6); }
constexpr int gw(int u) { return u == 1 ? C_LB : (u == 4 ? C_LC : (u == 6 ? C_LD : (u == 7 ? C_LE : -1))); }
constexpr int gs(int u) { return gn(u) + 1 + (u >= 6 || gw(u) >= 0); }
struct Seq { int k[176], i[176], go[44][4], g1[44], gc[8]; };
constexpr Seq mkseq() {
  Seq q{};
  int n = 0;
  const int kd[4] = {KD0, 44, KD2, KD3};
  for (int k = 0; k < 44; ++k)
    for (int i = 0; i < 4; ++i)
      if (k < kd[i]) q.k[n] = k, q.i[n++] = i;
  for (int c = 2; c < 5; ++c)
    for (int k = kd[c & 3]; k < 44; ++k) q.k[n] = k, q.i[n++] = c & 3;
  for (n = 175; n >= 0; --n) q.go[q.k[n]][q.i[n]] = q.g1[q.k[n]] = 64 + n;
  for (int u = 7; u >= 0; --u) {
    q.gc[u] = q.g1[gk(u)] - gs(u) - (gw(u) >= 0 ? PD : 0);
    if (u < 7 && q.gc[u] > q.gc[u + 1] - gs(u)) q.gc[u] = q.gc[u + 1] - gs(u);
  }
  return q;
}
constexpr Seq SQ = mkseq();
constexpr int gapof(int k, int i) { return SQ.go[k][i]; }
constexpr int gap1(int k) { return SQ.g1[k]; }
constexpr int gchk(int u) { return SQ.gc[u]; }
constexpr int gst(int v, int g0, int g1) { return v > 32 ? g1 - 34 + v : g0 + v + v * (g1 - g0 - 34) / 32; }
constexpr int gend(int v, int g0, int g1) { return gst(v + 1, g0, g1) - 1; }
constexpr int gstep(int g, int g0, int g1) { int v = -1; while (g < g1 && gst(v + 1, g0, g1) <= g) ++v; return v; }
enum { E_ST, E_LD, E_XS, E_XL };
struct Ev { int gap, kind, arg; };
struct EvTab { Ev e[96]; int n; };
#define LGX 57, 58, 84, 85, 86,  59, 60, 90, 92, 94,  61, 62, 95, 96, 97
#define S7D 4
#define S7G 67
#define CE 1
#define PC0 PC
#define FPK 1
#define LBL -1
#define SD 5
#define LA 101
#define LZ 20
#define EA 0
constexpr int LG[15] = {LGX};
constexpr int gpack(int i) { return gapof(43, i) + 2; }
constexpr EvTab mkev() {
  EvTab t{};
  for (int a = 0; a < 12; ++a)
    if (a != 2) t.e[t.n++] = Ev{(a < 6 ? gend(16 * (a / 3) + 17, GA0, GA1) : gend(16 * (a / 3 - 2) + 17, GB0, GB1)) - 1 + EA + a % 3 + (a / 3 == 2 ? SD : 0), E_ST, a};
  for (int u = 0, c = 0; u < 8; ++u)
    for (int a = 0; a < gl(u); ++a, ++c) {
      const int g = LA + (c - 15) * (gchk(7) - LZ - LA) / 29, m = gchk(u) - LZ - (gl(u) - 1 - a);
      t.e[t.n++] = Ev{c < 15 ? LG[c] : (g < m ? g : m), E_LD, u * 16 + a};
    }
  for (int a = 3; a < 12; ++a) t.e[t.n++] = Ev{gpack(a / 3) + EA + (a / 3 >= FPK ? (a % 3 != 0) : 1 + 2 * (a % 3) - (a % 3 == 2)), E_XS, a};
  for (int a = 0; a < 6; ++a) t.e[t.n++] = Ev{GE - (a < 3 ? XL1 : XL2) + a % 3, E_XL, a};
  for (int a = 1; a < t.n; ++a)
    for (int c = a; c > 0 && t.e[c - 1].gap > t.e[c].gap; --c) {
      const Ev x = t.e[c];
      t.e[c] = t.e[c - 1];
      t.e[c - 1] = x;
    }
  return t;
}
constexpr EvTab EV = mkev();
constexpr int evfirst(int g) { int a = 0; while (a < EV.n && EV.e[a].gap < g) ++a; return a; }
constexpr int evfind(int kind, int arg) { int a = 0; while (EV.e[a].kind != kind || EV.e[a].arg != arg) ++a; return a; }
constexpr int chkat(int g) { for (int u = 0; u < 8; ++u) if (g >= gchk(u) && g < gchk(u) + gs(u)) return u; return -1; }
static_assert(gend(17, GA0, GA1) < gap1(1) && gend(33, GA0, GA1) < gap1(3) && GA1 <= GB0 && GA1 - GA0 >= 34 && GB1 - GB0 >= 34, "first unit");
static_assert(gend(9, GB0, GB1) < gap1(KT) && gend(17, GB0, GB1) < gap1(KT + 1) && GB0 >= 65, "second unit");
static_assert(KD0 >= 14 && KD3 >= 14 && KD2 >= 14 && KD2 <= 41 - EA && KD3 <= 41 - EA && FPK == 1 && KD0 <= 36, "late steps");
constexpr bool lgok() {
  for (int u = 0; u < 8; ++u)
    for (int a = 0; a < gl(u); ++a)
      if (evfind(E_LD, u * 16 + a) > evfind(E_LD, u * 16 + gl(u) - 1) || EV.e[evfind(E_LD, u * 16 + a)].gap >= gchk(u)) return false;
  return true;
}
static_assert(lgok(), "loads");
static_assert(evfind(E_ST, 11) < evfind(E_LD, 7 * 16) && EV.e[EV.n - 1].gap < GE, "the fourth tile leaves before its registers are reloaded");

typedef u32 u32x16 __attribute__((ext_vector_type(16)));
typedef u32 u32x8 __attribute__((ext_vector_type(8)));
#define GAS __attribute__((address_space(1)))
template <class V> DI u64 kp(V v, int i) { return (u64)v[2 * i] | ((u64)v[2 * i + 1] << 32); }

enum { TA_W2 = 0, TA_CB = 14, TA_GRO, TA_GRS, TA_C0, TA_C1, TA_C2, TA_O1, TA_O2, TA_O3, TA_D0, TA_HS, TA_CX, TA_XE, TA_G6, TA_TO, TA_TW, TA_LW2,
       TB_LD = 0, TB_ST = 17, TB_OFF = 21, TB_XS, TB_XI = 25, TB_WX, TB_AX, TB_OUT, TB_EP, T_W1 = 31 };
struct Row { u32 v[8][32]; };
constexpr Row mkrow(int b) {
  Row t{};
  const int g = b & 3, r = (b & 4) * 2 + (b >> 5), j = (b >> 3) & 3, s = 4 * g + j, blk = r * 4 + j;
  u64 ord = (u64)((j == 0 || j == 3) ? 0xD9 : 0x9D) << 48;
  for (int p = 0; p < 12; ++p)
    ord |= ((0xCBA876354210ull >> (12 * (p < 3 ? j : j ^ (p < 9 ? (p - 1) / 2 : p - 8)) + 4 * (p < 3 ? p : (p < 9 ? (p - 3) & 1 : 2)))) & 15) << (4 * p);
  for (int rg = 0; rg < 4; ++rg) {
    u32 *a = t.v[rg], *c = t.v[4 + rg];
    const int hq = WS_HF + ((r * 4 + rg) * 56 + g * 14) * REC, hs = WS_HS + (r * 4 + rg) * 32 * REC, cx = 2 * j + (g >> 1);
    for (int p = 0; p < 14; ++p) a[TA_W2 + p] = (448 * g + rg * 8 + (int)((ord >> (4 * p)) & 15) * 32) * 1024 + j * 256;
    a[TA_CB] = (j * 128 + (((rg >> 1) ^ g) * 32) + (rg & 1) * 16) * 1024 + g * 256 + (j >> 1) * 128;
    a[TA_GRO] = WS_GR + (s * 16 + r) * 512;
    a[TA_GRS] = WS_GR + s * 8192;
    a[TA_C0] = (blk < 56 ? WS_GW + ((blk & 7) * 1792 + (7 * g + (blk >> 3)) * 64) * 8 : OOB) + rg * 128;
    a[TA_C1] = WS_GS + (r * 128 + s * 8 + rg * 2) * 8;
    a[TA_C2] = WS_GB + (s * 32 + r * 2) * 16 + rg * 8;
    a[TA_O1] = WS_GS + (r * 128 + rg * 32) * 8;
    a[TA_O2] = WS_GW + (14 * g + (int)((0xD923C832B751A640ull >> (16 * rg + 4 * j)) & 15)) * 256;
    a[TA_O3] = WS_GB + s * 512;
    a[TA_D0] = r - 1 - 4 * rg;
    a[TA_HS] = hs + s * 2 * REC;
    a[TA_CX] = cx;
    a[TA_XE] = RO[cx % 3] + rg * 4096 + (g & 1) * 64;
    a[TA_G6] = g << 6;
    a[TA_TO] = rg < 2 ? (g * 3584 + blk * 56 + rg * 24) * 128 : blk * 8192 + g * 256;
    a[TA_TW] = rg < 2 ? 0x1e07 : 0x0e09;
    a[TA_LW2] = rg * 2048;
    for (int u = 0; u < 11; ++u)
      c[TB_LD + u] = (u >= 3 && u < 6) ? hs + (s ^ (u - 2)) * 2 * REC : hq + (int)((ord >> (4 * (u < 3 ? 3 + 2 * u : 3 + u))) & 15) * REC;
    for (int i = 0; i < 4; ++i) c[TB_ST + i] = hq + (int)((ord >> (4 * (i < 3 ? i : 13))) & 15) * REC;
    c[TB_OFF] = j < 2 ? OOB : 0;
    for (int i = 1; i < 4; ++i) c[TB_XS + i - 1] = WS_X + ((((r * 4 + j) * 4 + (g ^ i)) * 4 + rg) * 3 + i - 1) * REC;
    c[TB_XI] = WS_X + (((r * 4 + j) * 4 + g) * 4 + rg) * 3 * REC;
    c[TB_WX] = j == 1 ? 64 : 0;
    c[TB_AX] = (j & 1) * 64 + 32768;
    c[TB_OUT] = ((r * 128 + rg * 32) * 512 + j * 128 + g * 32) * 4;
    c[TB_EP] = r == 15 && s == 0 && rg == 0;
    a[T_W1] = rg * 8 * 3584 + (7 * g + ((0x6411 >> (4 * j)) & 15)) * 128;
    c[T_W1] = a[T_W1] + 32 * 3584;
  }
  return t;
}
template <int B> constexpr Row ROW = mkrow(B);
#define R4(n) ROW<n>, ROW<n + 1>, ROW<n + 2>, ROW<n + 3>
#define R16(n) R4(n), R4(n + 4), R4(n + 8), R4(n + 12)
#define R64(n) R16(n), R16(n + 16), R16(n + 32), R16(n + 48)
static __device__ const Row TAB[256] __attribute__((aligned(128))) = {R64(0), R64(64), R64(128), R64(192)};
#undef R4
#undef R16
#undef R64

struct Ctx {
  u32x16 kw;
  int mso, vm[4];
  u32 tag, pa, pb, pc, pd, dv, bv;
  u32x4 ax_, aw_;
  u32x2 ab_;
  int stv;
  u64 sn[16];
};
template <int I, class V> DI i32x4 q4(V v) { return BC(i32x4, __builtin_shufflevector(v, v, I, I + 1, I + 2, I + 3)); }
#define PIECE(c, p)                                                                         \
  do {                                                                                      \
    if constexpr ((p) == 0) dma_s<RO[(c) % 3]>(q4<0>(cx.kw), cx.vm[0], cx.mso, cx.kw[7], cx.kw[8]); \
    else dma_n<(p) * 1024>(q4<0>(cx.kw), cx.vm[p], cx.mso);                                 \
  } while (0)
DI float fexp(float v) {
  const float a = v * 1.44269502f, e = __builtin_amdgcn_exp2f(a);
  return fmaf(e, fmaf(v, 1.92596303e-8f, fmaf(v, 1.44269502f, -a)) * 0.693147182f, e);
}

template <bool ISA> DI void role(const int w, int tid, Ctx& cx) {
  if constexpr (ISA) asm volatile("; A" : "+v"(tid));
  else asm volatile(".Lrole_b:" : "+v"(tid));
  STN(6);
  const int lane = tid & 63;
  const int rg = w & 3, h = lane >> 5, l31 = lane & 31;
  const int rl = lane >> 3, pos = lane & 7, gi = lane >> 4;
  u32x16 t0_, t1_, kd;
  u32x4 ke;
  u64 tp_;
  auto T = [&](int i) AI { return (int)(i < 16 ? t0_[i & 15] : t1_[i & 15]); };
  i32x4 rw, r1;
  const int ax = opq(l31 * 128 + (((h ^ (l31 >> 1)) & 7) << 4));
  const int tq = (lane >> 2) & 3, tp = lane & 3;
  int xa[4];
#pragma unroll
  for (int k = 0; k < 4; ++k) xa[k] = opq((ax ^ (k * 32)) + rg * 4096);
  int l16, sv, svp, vw1;
  const int trb = MI_TR + rg * 4608, lw1 = w * 1024;
  auto ids2 = [&]() AI {
    const int ln = opq(lane);
    sv = opq(ln == 0 ? MI_CNT : MI_JUNK + ln * 4);
    svp = opq(sv + rg * 16);
  };
  auto ids4 = [&]() AI {
    const int ln = opq(lane);
    vw1 = opq((ln >> 3) * 3584 + (((ln & 7) ^ ((ln >> 2) & 4)) << 4));
  };
  auto done = [&]() AI {
  };
  u32 tlo;
  cf lF;
  float lr, li, dv, bv;
  u32 pd;
  auto stats = [&]() AI {
    const int s0 = opqs(MI_PW + 512 + (w >> 1) * 1024 + (w & 1) * 32), s1 = opqs(MI_PW + 576 + ((w & 2) + (w >> 2)) * 1024 + (w & 1) * 4);
    int pw = MI_PW + w * 4 + ((lane & 6) * 2 + (lane & 1)) * 256 + gi * 64, ps = (gi & 1) ? s1 : s0;
    pin(pw, ps);
    f32x2 kf;
    int ta_;
    if constexpr (ISA) {
      const int ka = opqs(cx.kw[11]), kb = opqs(cx.kw[12]);
      kf = f32x2{(float)(rg * 512 + h * 128), asf(h ? kb : ka)};
      ta_ = MI_LT + tid * 8;
      pin(kf, ta_);
    }
    STN(7);
    waitv<ISA ? 10 : 4>();
    STN(1);
    {
      float sm, sq, bs, y[8];
      asm volatile("v_dot2_f32_bf16 %0, %3, %9, 0\n\tv_dot2_f32_bf16 %1, %3, %3, 0\n\tv_dot2_f32_bf16 %2, %7, %9, 0\n\t"
                   "v_dot2_f32_bf16 %0, %4, %9, %0\n\tv_dot2_f32_bf16 %1, %4, %4, %1\n\tv_dot2_f32_bf16 %2, %8, %9, %2\n\t"
                   "v_dot2_f32_bf16 %0, %5, %9, %0\n\tv_dot2_f32_bf16 %1, %5, %5, %1\n\t"
                   "v_dot2_f32_bf16 %0, %6, %9, %0\n\tv_dot2_f32_bf16 %1, %6, %6, %1"
                   : "=&v"(sm), "=&v"(sq), "=&v"(bs)
                   : "v"(cx.ax_.x), "v"(cx.ax_.y), "v"(cx.ax_.z), "v"(cx.ax_.w), "v"(cx.ab_.x), "v"(cx.ab_.y), "s"(0x3f803f80u));
#pragma unroll
      for (int i = 0; i < 4; ++i)
        asm volatile("v_lshlrev_b32 %0, 16, %2\n\tv_and_b32 %1, 0xffff0000, %2" : "=&v"(y[2 * i]), "=v"(y[2 * i + 1]) : "v"(cx.aw_[i]));
      pin(sm, sq, bs);
      SB;
      sm = add32(sm, sq);
      bs = add32(bs, bs);
#pragma unroll
      for (int i = 0; i < 4; ++i) y[i] = add32(y[i], y[i + 4]);
      pin(sm, bs, y[0], y[1], y[2], y[3]);
      SB;
      sm = add16(sm, bs);
      y[0] = add16(y[0], y[2]);
      y[1] = add16(y[1], y[3]);
      pin(sm, y[0], y[1]);
      sm = dppadd<0xB1>(sm);
      y[0] = dppadd<0x128>(y[0]);
      pin(y[0]);
      y[1] = dppadd<0x128>(y[1]);
      pin(y[1]);
      sm = dppadd<0x4E>(sm);
      pin(sm);
      LDS(float, pw) = y[0];
      LDS(float, pw + 32) = y[1];
      SB;
      sm = dppadd<0x140>(dppadd<0x141>(sm));
      halves(sm, bs, sq);
      const float m = bs * (1.0f / 512.0f), v = fmaxf(fmaf(-m, m, sq * (1.0f / 512.0f)), 0.f);
      u32 val = pk2(m, __builtin_amdgcn_sqrtf(rbf(rbf(v) + 1e-5f)));
      pin(val);
      LDS(u32, ps) = (gi & 1) ? asu(sm) : val;
    }
    SB;
    PIECE(1, 0); PIECE(1, 1); PIECE(1, 2); PIECE(1, 3);
    STN(2);
    if constexpr (ISA) {
      float lg;
      asm volatile("s_waitcnt vmcnt(8)\n\tv_lshlrev_b32 %0, 16, %3\n\tv_lshlrev_b32 %1, 16, %4\n\tv_lshlrev_b32 %2, 16, %5"
                   : "=&v"(lg), "=&v"(lr), "=&v"(li)
                   : "v"(cx.pc), "v"(cx.pa), "v"(cx.pb)
                   : "memory");
      asm volatile("v_lshlrev_b32 %0, 16, %3\n\tv_lshlrev_b32 %1, 16, %4\n\tv_mov_b32 %2, %5"
                   : "=&v"(dv), "=&v"(bv), "=&v"(pd)
                   : "v"(cx.dv), "v"(cx.bv), "v"(cx.pd));
      STN(8);
      const f32x2 dt = rbf(fexp(lg)) * kf, th = dt * li;
      float sn[2], cs[2];
      auto F = [](f32x2 a, f32x2 b, f32x2 c) AI { return __builtin_elementwise_fma(a, b, c); };
      auto K = [](float c) AI { return f32x2{c, c}; };
      const f32x2 t2 = th * 0.636619772f, n = {__builtin_rintf(t2.x), __builtin_rintf(t2.y)}, v = dt * lr, va = v * 1.44269502f;
      const f32x2 y = F(n, K(1.71512451e-15f), F(n, K(4.37113883e-8f), F(n, K(-1.57079637f), th))), z = y * y;
      const f32x2 ex = {__builtin_amdgcn_exp2f(va.x), __builtin_amdgcn_exp2f(va.y)};
      const f32x2 pc_ = F(z * z, F(z, F(z, K(2.44331571e-5f), K(-1.38873163e-3f)), K(4.16666457e-2f)), F(z, K(-0.5f), K(1.0f)));
      const f32x2 ps_ = F(z * y, F(z, F(z, K(-1.95152959e-4f), K(8.33216087e-3f)), K(-1.66666546e-1f)), y);
      const f32x2 er = F(ex, F(v, K(1.92596303e-8f), F(v, K(1.44269502f), -va)) * 0.693147182f, ex);
#pragma unroll
      for (int i = 0; i < 2; ++i) {
        const u32 qd = (u32)(int)n[i];
        sn[i] = asf(asu((qd & 1) ? pc_[i] : ps_[i]) ^ ((qd & 2) << 30)), cs[i] = asf(asu((qd & 1) ? ps_[i] : pc_[i]) ^ (((qd + 1) & 2) << 30));
      }
      float e0 = er.x, e1 = er.y;
      pin(sn[0], sn[1], cs[0], cs[1], e0, e1);
      if (__builtin_expect(__builtin_amdgcn_ballot_w64(!(fmaxf(fabsf(th.x), fabsf(th.y)) < 65536.f)) != 0, 0)) {
        _Pragma("nounroll") for (int i = 0; i < 2; ++i) {
          sn[0] = sn[1], cs[0] = cs[1];
          sincosf(i ? th.y : th.x, &sn[1], &cs[1]);
        }
      }
      lF = cf{e0 * cs[0], e0 * sn[0]};
      LDS(f32x2, ta_) = cf{e1 * cs[1], e1 * sn[1]};
      SB;
      STN(9);
    }
  };

  auto bar0 = [&]() AI {
    asm volatile("s_waitcnt vmcnt(4) lgkmcnt(0)\n\ts_add_u32 %0, %6, 1\n\ts_barrier"
                 : "=&s"(tlo), "={s[48:63]}"(t0_), "={s[64:79]}"(t1_), "={s[80:95]}"(kd), "={s[96:99]}"(ke), "={s[46:47]}"(tp_)
                 : "s"(cx.tag)
                 : "memory", "scc");
    rw = q4<0>(kd), r1 = q4<4>(kd);
    STF(1); STF(2); STF(10); STF(11);
    if constexpr (ISA) { STF(8); STF(9); }
  };
  auto w1p = [&](auto Cc) AI {
    constexpr int c = decltype(Cc)::v;
    dma1<c * 64 * 3584, U1S[c] * 8192>(r1, vw1, T(T_W1), lw1);
  };

  if constexpr (ISA) {
    cf l1, l2, l3, l4, l8, l32, l256, co2, lL, lH, mq[4];
    f32x16 are = {}, aim = {};
    bf16x8 fxr[3], fbr[3], fbi[3];
    int xb[4];
#pragma unroll
    for (int k = 0; k < 4; ++k) xb[k] = opq((ax ^ (k * 32)) + WB);
    auto rdi = [&](int n) AI {
      fbi[n % 3] = ld128(xb[n & 3] + RO[(n >> 2) % 3] + 4096); };
    auto rdx = [&](int n) AI {
      fbr[n % 3] = ld128(xb[n & 3] + RO[(n >> 2) % 3]);
      fxr[n % 3] = ld128(xa[n & 3] + RO[(n >> 2) % 3]);
    };
    int vw2, vw3, d0;
    auto w2t = [&](auto Pp) AI {
      constexpr int p = decltype(Pp)::v;
      dma2<W2S[p] * 8192>(q4<8>(kd), vw2, vw3, T(TA_W2 + p), T(TA_LW2));
    };
    auto ctile = [&](auto Pp) AI {
      constexpr int p = decltype(Pp)::v;
      dma2<(4 + p) * 8192>((p & 2) ? q4<0>(ke) : q4<12>(kd), cx.vm[0], cx.vm[1], opqs(T(TA_CB)) ^ ((p & 1) * 65536 + (p >> 2) * 128), T(TA_LW2));
    };
    float t0, t1;
    int o1, o2, o3, a8, aw, at, lo0, lo1, ap, aq, ta;
    u32x2 gs;
    u32x4 gw0, gw1, gb;
    u32 nm;
    f32x4 rsv[4], nmv[4];
    int tch;
    int cvo;
    auto gather = [&]() AI {
      asm volatile("buffer_load_dwordx2 %0, %4, %7, %8 offen sc1\n\tbuffer_load_dwordx4 %1, %5, %7, %9 offen sc1\n\t"
                   "buffer_load_dwordx4 %2, %5, %7, %9 offen offset:16 sc1\n\tbuffer_load_dwordx4 %3, %6, %7, %10 offen sc1"
                   : "=&v"(gs), "=&v"(gw0), "=&v"(gw1), "=&v"(gb)
                   : "v"(o1), "v"(o2), "v"(o3), "s"(rw), "s"(T(TA_O1)), "s"(T(TA_O2)), "s"(T(TA_O3))
                   : "memory");
    };
    int po, pa, ca, ra, rb, xe;
    f32x4 p0, p1;
    u32x2 xv[4];
    asm volatile("" : "=v"(xv[0]), "=v"(xv[1]), "=v"(xv[2]), "=v"(xv[3]));
    l16 = opq(lane * 16);
    o1 = opq(l31 * 8);
    o3 = opq(l31 * 16);
    o2 = opq(rl * 1792 * 8 + pos * 32);
    po = opq(lane < 16 ? o1 : OOB);
    pa = opq(MI_PW + rg * 1024 + l31 * 32);
    a8 = opq(MI_LT + o1);
    xe = opq((4 * h + tq) * 128 + ((((gi & 1) * 2 + (tp >> 1)) ^ (2 * h + (tq >> 1))) << 4) + (tp & 1) * 8);
    ids2();
    ids4();
    auto prep = [&](auto Qq) AI {
      constexpr int q = decltype(Qq)::v;
      if constexpr (q == 0) xe = opq(xe ^ T(TA_XE));
      if constexpr (q == QP - 2) lanes3(po, T(TA_C0), T(TA_C1), T(TA_C2));
      if constexpr (q == 26) {
        ap = opq(trb + opq(lane) * 8);
        LDS(f32x2, ap) = f32x2{0.f, 0.f};
        aq = opq((ap ^ 256) + h * 512);
      }
      if constexpr (q == 27) {
        ra = opq((rg * 32 + opq(l31)) * 4);
        aw = opq(a8 + opq(h) * 4 + rg * 256);
      }
      if constexpr (q == 30) {
        at = opq(trb + opq(l31) * 72 + h * 8);
        cvo = opq(opq(lane) < (w == 3 ? 32 : 0) ? l16 : OOB);
      }
      if constexpr (q == 32) {
        d0 = opq(T(TA_D0) - opq(h));
        lo0 = opq(d0 * 512 + o3);
        lo1 = opq(d0 >= 2 ? lo0 - 1024 : OOB);
      }
      if constexpr (q == 35) {
        lo0 = opq(d0 >= 0 ? lo0 : OOB);
        rb = opq(MI_RS + rg * 128 + opq(h) * 16);
      }
      if constexpr (q == 38) {
        vw2 = opq(((opq(gi) * 1088) ^ (l16 & 255)) ^ T(TA_G6));
        vw3 = opq(vw2 + 3072);
      }
      if constexpr (q == 40) ta = opq(trb + (8 * opq(h) + tq) * 72 + 32 * (gi & 1) + 8 * tp);
      if constexpr (q == 42) ca = opq(MI_CT + rg * 256 + opq(pos) * 32 + gi * 8);
      if constexpr (q == QT - 1) tch = opq((((opq(lane) & (T(TA_TW) >> 8)) << (T(TA_TW) & 31)) | ((lane & 1) << 7)) + T(TA_TO));
    };
    auto side = [&](auto Qq) AI {
      constexpr int q = decltype(Qq)::v;
      prep(Qq);
      if constexpr (q % 8 == 0) xread<q / 8>(xv, xe, T(TA_CX));
      if constexpr (q == QP - 3) {
        p0 = LDS(f32x4, pa);
        p1 = LDS(f32x4, pa + 16);
      }
      if constexpr (q == QP - 1) {
        p0.y += p0.x;
        pin(p0);
        p0.z += p0.w;
        pin(p0);
        p1.x += p1.y;
        pin(p1);
        p1.z += p1.w;
        pin(p1);
        if (lane < 16) p0.x = (p0.y + p0.z) + (p1.x + p1.z);
        pin(p0);
      }
      if constexpr (q == QP) stg2<0>(u32x2{lane < 18 ? asu(p0.x) : split2(p0.y), tlo}, po, rw);
      if constexpr (q == QL + 6) {
        l1 = LDS(f32x2, a8);
        l2 = LDS(f32x2, a8 + 256);
        l4 = LDS(f32x2, a8 + 2 * 256);
        l8 = LDS(f32x2, a8 + 3 * 256);
        t0 = fmaf(lr, lr, li * li);
        t1 = __builtin_amdgcn_rcpf(t0);
        pin(t0, t1);
      }
      if constexpr (q == QL + 8) {
        l32 = LDS(f32x2, a8 + 4 * 256);
        l256 = LDS(f32x2, a8 + 7 * 256);
        lL = LDS(f32x2, a8 + 768 + rg * 256);
        t0 = fmaf(fmaf(-t0, t1, 1.0f), t1, t1);
        t0 += t0;
        pin(t0);
      }
      if constexpr (q == QL + 11) {
        lr *= t0;
        pin(lr);
        li *= -t0;
        pin(li);
        co2 = cmul(cf{l1.x - 1.0f, l1.y}, cf{lr, li});
        pin(co2);
      }
      if constexpr (q == QL + 14) {
        l3 = cmul(l1, l2);
        lH = csel(h, cf{1.f, 0.f}, l4);
        pin(l3, lH);
      }
      if constexpr (q == QL + 16) {
        mq[0] = csel(h, cmul(co2, l4), co2);
        pin(mq[0]);
      }
      if constexpr (q == QL + 19) {
        mq[1] = cmul(mq[0], l8);
        lL = csel(rg, lL, cf{1.f, 0.f});
        pin(mq[1], lL);
      }
      if constexpr (q == QL + 22) {
        mq[2] = cmul(mq[1], l8);
        pin(mq[2]);
      }
      if constexpr (q == QL + 24) {
        mq[3] = cmul(mq[2], l8);
        pin(mq[3]);
      }
      if constexpr (q == QG) gather();
      if constexpr (q == QT) touch(tch, tp_);
      if constexpr (q == QC) {
        asm volatile("s_waitcnt vmcnt(%4)" : "+v"(gs), "+v"(gw0), "+v"(gw1), "+v"(gb) : "n"(asince(QG + 1, QC + 1) + (QT == QG)) : "memory");
        auto stale = [&]() AI {
          return __builtin_amdgcn_ballot_w64((((gs.y ^ tlo) | (gw0.y ^ tlo)) | ((gw0.w ^ tlo) | (gw1.y ^ tlo)) | ((gw1.w ^ tlo) | (gb.y ^ tlo)) |
                                              (gb.w ^ tlo)) != 0) != 0;
        };
        if (__builtin_expect(stale(), 0)) {
          _Pragma("nounroll") for (u32 it = 0; it < SPIN_CAP; ++it) {
            gather();
            asm volatile("s_waitcnt vmcnt(0)" : "+v"(gs), "+v"(gw0), "+v"(gw1), "+v"(gb)::"memory");
            if (!stale()) break;
          }
        }
      }
      if constexpr (q == QC + S1) {
        t0 = add32(asf(gw0.x), asf(gw1.x));
        t1 = add32(asf(gw0.z), asf(gw1.z));
        pin(t0, t1);
      }
      if constexpr (q == QC + S2) {
        t0 = dppadd<0x128>(add16(t0, t1));
        t1 = __builtin_amdgcn_rcpf(bfh(gs.x));
        nm = (gs.x & 0xffffu) ^ 0x8000u;
        pin(t0, t1, nm);
      }
      if constexpr (q == QC + S3) {
        t1 = fmaf(fmaf(-bfh(gs.x), t1, 1.0f), t1, t1);
        pin(t1);
        LDS(float, MI_RS + h * 512 + ra) = h ? bf2f(nm) * t1 : t1;
      }
      if constexpr (q == QC + S4) {
        const u32 p = split2(t0);
        LDS(u32x2, ca) = u32x2{pd | (p << 16), p >> 16};
      }
      if constexpr (q == QC + S5) {
        const float gc0 = -2.302208198f * t1;
        LDS(u32x4, MI_RC + ra * 4) = u32x4{asu(gc0), asu(gc0 * 0.044715f * (t1 * t1)), (gs.x >> 16) | (nm << 16), nm};
      }
      if constexpr (q == QC + S6) {
        nm = h ? 0u : nm | (nm << 16);
        gw0 = u32x4{nm, 0u, 0u, 0u};
        pin(gw0);
      }
      if constexpr (q == QC + S7) {
        gb.y = gb.w = gb.x;
        gw1 = u32x4{gb.z, gb.z, gb.z, gb.z};
        pin(gb, gw1);
      }
      if constexpr (q == QC + S8) {
        are = MFMA32(BC(bf16x8, gw0), BC(bf16x8, gb), are); pin(are);
        aim = MFMA32(BC(bf16x8, gw0), BC(bf16x8, gw1), aim); pin(aim);
      }
      if constexpr (q == 61) {
#pragma unroll
        for (int i = 0; i < 4; ++i) rsv[i] = LDS(f32x4, rb + i * 32);
      }
      if constexpr (q == 62) {
#pragma unroll
        for (int i = 0; i < 4; ++i) nmv[i] = LDS(f32x4, rb + 512 + i * 32);
      }
    };
    SB;
    stats();
    bar0();
    STAMP(3);
    rdi(0); rdx(0); rdi(1); rdx(1);
    sfor<0, 32>([&](auto Nn) AI {
      constexpr int n = decltype(Nn)::v, ks = n & 3;
      SB;
      are = MFMA32(fxr[n % 3], fbr[n % 3], are); pin(are);
      SB;
      if constexpr (ks == 2) bar<abar(n)>();
      else if (n + 2 < 32) {
        rdi(n + 2);
        if (ks == 1) rdx(n + 2);
      }
      if constexpr (n < 2) PIECE(2, 2 * n);
      SB;
      side(IC<2 * n>{});
      SB;
      aim = MFMA32(fxr[n % 3], fbi[n % 3], aim); pin(aim);
      SB;
      if (n + 2 < 32) {
        if (ks == 2) rdi(n + 2);
        if (ks != 1) rdx(n + 2);
      }
      if constexpr (n < 2) PIECE(2, 2 * n + 1);
      else if constexpr (n < 22) PIECE(((n - 2) >> 2) + 3, (n - 2) & 3);
      else if constexpr (n < 30) w1p(IC<n - 22>{});
      else w2t(IC<n - 30>{});
      SB;
      side(IC<2 * n + 1>{});
    });
    SB;
    STAMP(4);
    PRIO(2);
    pin(rsv[0], rsv[1], rsv[2], rsv[3]);
    cf u[16], sq[4], pq[4];
#pragma unroll
    for (int i = 0; i < 16; ++i) {
      float a = are[i] * rsv[i >> 2][i & 3], c = aim[i] * rsv[i >> 2][i & 3];
      pin(a, c);
      u[i] = cf{a, c};
    }
    {
#pragma unroll
      for (int k = 1; k < 4; ++k)
#pragma unroll
        for (int q = 0; q < 4; ++q) u[4 * q + k] = cfma(l1, u[4 * q + k - 1], u[4 * q + k]);
      pq[0] = u[3];
#pragma unroll
      for (int q = 1; q < 4; ++q) pq[q] = cfma3(l8, pq[q - 1], u[4 * q + 3]);
      cf Tt = cmul(lH, pq[3]);
      settle(Tt);
      LDS(float, aw + (MI_WT - MI_LT)) = add32(Tt.x, Tt.y);
    }
    cf t4[4];
    auto meet = [&](auto Oo, int a) AI {
      constexpr int o = decltype(Oo)::v;
      sig<o>(sv);
      _Pragma("nounroll") for (u32 it = 0; it < (1u << 22); ++it) {
        const u32 c = peek<o>(sv);
#pragma unroll
        for (int i = 0; i < 4; ++i) t4[i] = *(volatile LAS f32x2*)(size_t)(a + i * 256);
        if ((u32)__builtin_amdgcn_readfirstlane(c) >= 4u) break;
      }
    };
    meet(IC<C_WT>{}, a8 + (MI_WT - MI_LT));
    cf lin;
    {
      const cf s1 = cfma(l32, t4[0], t4[1]), s2 = cfma(l32, s1, t4[2]), s3 = cfma(l32, s2, t4[3]);
      stg4<0>(u32x4{asu(s3.x), tlo, asu(s3.y), tlo}, cvo, rw, T(TA_GRO));
      asm volatile("s_nop 1");
      PRIO(0);
      lin = csel(rg & 2, csel(rg & 1, s2, s1), csel(rg & 1, t4[0], cf{0.f, 0.f}));
    }
    STA(5);
    asm volatile("s_waitcnt vmcnt(5)" : "+v"(tch)::"memory");
    sig<C_U1>(sv);
#pragma unroll
    for (int q = 0; q < 4; ++q) *(volatile LAS f32x2*)(size_t)(ap + 512 + q * 512) = pq[q];
#pragma unroll
    for (int q = 0; q < 4; ++q) sq[q] = *(volatile LAS f32x2*)(size_t)(aq + q * 512);
    ctile(IC<0>{}); ctile(IC<1>{});
    PRIO(2);
    pin(co2);
#pragma unroll
    for (int i = 0; i < 16; ++i) u[i] = cmul(co2, u[i]);
#pragma unroll
    for (int q = 0; q < 4; ++q) sq[q] = cmul(co2, q ? cfma(l4, pq[q - 1], sq[q]) : sq[q]);
#pragma unroll
    for (int q = 0; q < 4; ++q) pin(u[4 * q], u[4 * q + 1], u[4 * q + 2], u[4 * q + 3], sq[q]);
    pin(lin);
    PRIO(0);
    ctile(IC<2>{}); ctile(IC<3>{});
    w2t(IC<2>{}); w2t(IC<3>{}); w2t(IC<4>{});
    u32x4 lb0, lb1;
    auto look = [&](int at) AI {
      if (at == LBL) {
        ldg4(lb0, lo0, rw, T(TA_GRS));
        ldg4(lb1, lo1, rw, T(TA_GRS));
      }
    };
    look(-1);
    lwait<C_P1, 4>(sv);
    look(0); w2t(IC<5>{}); look(1); w2t(IC<6>{}); look(2); w2t(IC<7>{}); look(3); w2t(IC<8>{}); look(4);
    STA(6);
    asm volatile("s_waitcnt vmcnt(%2)" : "+v"(lb0), "+v"(lb1) : "n"(LBL < 0 ? 8 : 8 - 2 * LBL) : "memory");
    sig<C_LA>(sv);
    PRIO(2);
    _Pragma("nounroll") for (u32 it = 0; it < SPIN_CAP; ++it) {
      const bool bad = ((d0 >= 0) & (((lb0.y ^ tlo) | (lb0.w ^ tlo)) != 0)) | ((d0 >= 2) & (((lb1.y ^ tlo) | (lb1.w ^ tlo)) != 0));
      if (__builtin_amdgcn_ballot_w64(bad) == 0) break;
      ldg4(lb0, lo0, rw, T(TA_GRS));
      ldg4(lb1, lo1, rw, T(TA_GRS));
      asm volatile("s_waitcnt vmcnt(0)" : "+v"(lb0), "+v"(lb1)::"memory");
    }
    {
      cf a = cmul(lF, cfma(l256, cf{asf(lb1.x), asf(lb1.z)}, cf{asf(lb0.x), asf(lb0.z)}));
      settle(a);
      LDS(float, aw + (MI_CT - MI_LT)) = add32(a.x, a.y);
    }
    if (CE == 1) {
      lwait<C_P5, 4>(sv);
      ctile(IC<6>{}); ctile(IC<7>{});
    }
    meet(IC<C_HP>{}, a8 + (MI_CT - MI_LT));
    STA(7);
    {
      float zr, zi, z0, z1;
      asm("v_add_f32 %0, %4, %6\n\tv_add_f32 %1, %5, %7\n\tv_add_f32 %2, %8, %10\n\tv_add_f32 %3, %9, %11\n\tv_add_f32 %0, %0, %2\n\tv_add_f32 %1, %1, %3"
          : "=&v"(zr), "=&v"(zi), "=&v"(z0), "=&v"(z1)
          : "v"(t4[0].x), "v"(t4[0].y), "v"(t4[1].x), "v"(t4[1].y), "v"(t4[2].x), "v"(t4[2].y), "v"(t4[3].x), "v"(t4[3].y));
      const cf z = cfma3(lL, cf{zr, zi}, lin);
      cf t[4];
#pragma unroll
      for (int q = 0; q < 4; ++q) t[q] = cfma3(mq[q], z, sq[q]);
#pragma unroll
      for (int q = 0; q < 4; ++q) {
        u32 r0, r1, i0, i1;
        fin2(l1, l2, t[q], u[4 * q], u[4 * q + 1], r0, i0);
        fin2(l3, l4, t[q], u[4 * q + 2], u[4 * q + 3], r1, i1);
        LDS(u32x2, at + 16 * q) = u32x2{r0, r1};
        LDS(u32x2, at + 2304 + 16 * q) = u32x2{i0, i1};
      }
    }
    sig<C_PAIR>(svp);
    if (CE == 2) {
      lwait<C_P2, 4>(sv);
      ctile(IC<4>{}); ctile(IC<5>{}); ctile(IC<6>{}); ctile(IC<7>{});
    }
    {
      u32x4 f[4];
#pragma unroll
      for (int i = 0; i < 4; ++i) f[i] = BC(u32x4, ldtr(ta + (i >> 1) * 2304 + (i & 1) * 16 * 72, 4 * 72));
      const int so = T(TA_HS);
      stg4<0>(f[0], l16, rw, so);
      stg4<1024>(f[1], l16, rw, so);
      stg4<2304>(f[2], l16, rw, so);
      stg4<3328>(f[3], l16, rw, so);
      stg1(csum(f[0], f[1], f[2], f[3], tlo), lane * 4, rw, so);
      pin(f[0], f[1], f[2], f[3]);
    }
    PRIO(0);
    STA(8);
    waitv<5 + 4 * CE>();
    sig<C_LB>(sv);
    lwait<C_P2, 4>(sv);
    if (CE < 2) { waitv<PC0>(); ctile(IC<4>{}); waitv<PC0>(); ctile(IC<5>{}); }
    if (CE < 1) { waitv<PC0>(); ctile(IC<6>{}); waitv<PC0>(); ctile(IC<7>{}); }
    STA(9);
    lwait<C_P4, 4>(sv);
    waitv<PC>(); w2t(IC<9>{});
    waitv<(PC < 2 ? PC : 2)>();
    sig<C_LC>(sv);
    STA(10);
    w2t(IC<10>{}); waitv<PC>(); w2t(IC<11>{});
    lwait<C_P3, 4>(sv);
    STA(11);
    waitv<PC>(); w2t(IC<12>{});
    waitv<(PC < 2 ? PC : 2)>();
    sig<C_LD>(sv);
    w2t(IC<13>{});
    SB;
    {
      f32x4 ep[4];
#pragma unroll
      for (int e = 0; e < 16; ++e) {
        const u32 xw = xv[e >> 2][(e >> 1) & 1];
        float xf = (e & 1) ? bfh(xw) : bf2f(xw), xn = fmaf(xf, rsv[e >> 2][e & 3], nmv[e >> 2][e & 3]);
        pin(xn);
        xf += bv;
        pin(xf);
        xf = fmaf(xn, dv, xf);
        pin(xf);
        ep[e >> 2][e & 3] = xf;
      }
#pragma unroll
      for (int q = 0; q < 4; ++q) LDS(f32x4, trb + q * 1024 + l16) = ep[q];
    }
    waitv<0>();
    sig<C_LE>(sv);
    done();
  } else {
    f32x16 a0[2] = {}, a1[2];
    bf16x8 xr[32], fw[3][2];
    int wa[2];
    {
      const int aw0 = (8 * h + tq) * 128 + ((tq >> 1) << 6) + (gi & 1) * 32 + tp * 8;
      wa[0] = opq(aw0 + WB);
      wa[1] = opq((aw0 ^ 64) + WB);
    }
    auto rdw = [&](int n, int t) AI {
      if (n == 32) wa[t] = opq((wa[t] + 16384) ^ T(TB_WX));
      fw[n % 3][t] = ldtr(wa[t] + (n < 32 ? RO[(n >> 2) % 3] + 8192 : (U1S[(n >> 2) & 7] - 8) * 8192) + (n & 3) * 2048, 512);
    };
    auto rdx = [&](int n) AI { xr[n] = ld128(xa[n & 3] + RO[(n >> 2) % 3]); };
    u32x4 rc;
    float sd;
    u32x2 ct[4];
    u32x4 hf[44];
    float gz, ge, gr, go[2];
    u32 cso[2], hc[11], c1, c2;
    auto gelu = [&](const f32x16* a, auto Vv, auto Pp, auto Oo) AI {
      constexpr int v = decltype(Vv)::v, P = decltype(Pp)::v, o = decltype(Oo)::v;
      constexpr bool h1 = P != 2, h2 = P != 1, s0 = v < 32, s1 = v >= 1 && v < 33, s2 = v >= 2;
      constexpr bool cv_ = h2 && v >= 3 && (v & 1), ad = v == 33 || (h1 && v >= 6 && v % 4 == 2);
      constexpr int i = v >= 3 ? (v - 3) / 2 : 0, f = i >> 2, z = o ? i >> 3 : 1;
      constexpr bool first = (i & (o ? 7 : 15)) == 1;
      u32x4& d = hf[o ? (f < 2 ? KT + f : 40 + f) : f];
      u32 p;
      constexpr int vx = s0 ? v : 31, vp = s2 ? v - 2 : 0;
#define GI(c, t) ".if " c "\n\t" t "\n\t.endif\n\t"
#define GTXT                                                                                                                     \
  GI("%c[m] & 1", "v_mul_f32 %[z], %[x], %[x]") GI("%c[m] & 2", "v_fma_f32 %[e], %[e], %[sd], %[sd]")                             \
  GI("%c[m] & 4", "v_fma_f32 %[z], %[c1], %[z], %[c0]") GI("%c[m] & 8", "v_mul_f32 %[o], %[xp], %[r]")                           \
  GI("(%c[m] & 12) == 12", "v_mul_f32 %[z], %[x], %[z]") GI("%c[m] & 16", "v_cvt_pk_bf16_f32 %[p], %[op], %[o]")                 \
  GI("(%c[m] & 12) == 4", "v_mul_f32 %[z], %[x], %[z]")
#define GEND GI("%c[m] & 2", "v_rcp_f32 %[r], %[e]") GI("%c[m] & 4", "v_exp_f32 %[e], %[z]")
#define GOUT [z] "+v"(gz), [e] "+v"(ge), [r] "+v"(gr), [o] "+v"(go[v & 1])
#define GIN [x] "v"(a[vx >> 4][vx & 15]), [xp] "v"(a[vp >> 4][vp & 15]), [op] "v"(go[~v & 1]), [sd] "v"(sd), [c0] "v"(rc.x), [c1] "v"(rc.y), [m] "n"(m)
      constexpr int m = (h1 && s0) | (h1 && s1) << 1 | (h2 && s0) << 2 | (h1 && s2) << 3 | cv_ << 4;
      if constexpr (ad && first) asm volatile(GTXT "v_add3_u32 %[c], %[p], %[q], %[t]\n\t" GEND : GOUT, [c] "=&v"(cso[z]) : GIN, [p] "v"(d.y), [q] "v"(d.x), [t] "s"(tlo));
      else if constexpr (ad && cv_) asm volatile(GTXT "v_add3_u32 %[c], %[p], %[q], %[c]" : GOUT, [p] "=&v"(p), [c] "+v"(cso[z]) : GIN, [q] "v"(d.z));
      else if constexpr (ad) asm volatile(GTXT "v_add3_u32 %[c], %[p], %[q], %[c]\n\t" GEND : GOUT, [c] "+v"(cso[z]) : GIN, [p] "v"(d[i & 3]), [q] "v"(d[(i & 3) - 1]));
      else if constexpr (cv_) asm volatile(GTXT GEND : GOUT, [p] "=&v"(p) : GIN);
      else asm volatile(GTXT GEND : GOUT : GIN, [p] "v"(sd));
      if constexpr (cv_) d[i & 3] = p;
    };
    int rca, cta;
    l16 = opq(lane * 16);
    const int l4 = opq(lane * 4);
    LDS(u32, MI_CNT + (l4 & 127)) = 0u;
    ids2();
    ids4();
    auto dmab = [&](auto Qq) AI {
      constexpr int q = decltype(Qq)::v, i = bdma(q);
      if constexpr (q < 4) PIECE(2, q);
      else if constexpr (i >= 20) w1p(IC<i - 20>{});
      else if constexpr (i >= 0) PIECE((i >> 2) + 3, i & 3);
      if constexpr (q == 31) {
        rca = opq(MI_RC + rg * 512 + (opq(l16) & 511));
        cta = opq(MI_CT + opq(l31) * 8);
      }
    };
    SB;
    stats();
    bar0();
    STAMP(3);
    rdw(0, 1); rdw(0, 0); rdx(0); rdw(1, 1); rdw(1, 0); rdx(1);
    sfor<0, 32>([&](auto Nn) AI {
      constexpr int n = decltype(Nn)::v, ks = n & 3;
      SB;
      a0[0] = MFMA32(fw[n % 3][0], xr[n], a0[0]); pin(a0[0]);
      SB;
      if (ks != 2) rdw(n + 2, 1);
      if (ks == 1) {
        rdw(n + 2, 0);
        rdx(n + 2);
      }
      dmab(IC<2 * n>{});
      if (ks == 2) bar<4>();
      SB;
      a0[1] = MFMA32(fw[n % 3][1], xr[n], a0[1]); pin(a0[1]);
      SB;
      if (ks == 2) {
        STS(4, (5 + n / 4));
        if (n == 30) {
          rc = LDS(u32x4, rca);
#pragma unroll
          for (int t = 0; t < 4; ++t) ct[t] = LDS(u32x2, cta + t * 256);
        }
        rdw(n + 2, 1);
      }
      if (ks != 1) {
        rdw(n + 2, 0);
        if (n + 2 < 32) rdx(n + 2);
      }
      dmab(IC<2 * n + 1>{});
    });
    SB;
    PRIO(1);
    {
      const bf16x8 cb = BC(bf16x8, (u32x4{h ? 0u : rc.z, h ? 0u : rc.w, 0u, 0u}));
      sd = bf2f(rc.z);
      a0[0] = MFMA32(BC(bf16x8, (u32x4{ct[0].x, ct[0].y, ct[1].x, ct[1].y})), cb, a0[0]); pin(a0[0]);
      a1[0] = MFMA32(BC(bf16x8, (u32x4{ct[2].x, ct[2].y, ct[3].x, ct[3].y})), cb, f32x16{}); pin(a1[0]);
      const bf16x8 cc = BC(bf16x8, (u32x4{0u, 0u, h ? 0u : rc.z, h ? 0u : rc.w}));
      a0[1] = MFMA32(BC(bf16x8, (u32x4{ct[0].x, ct[0].y, ct[1].x, ct[1].y})), cc, a0[1]); pin(a0[1]);
      a1[1] = MFMA32(BC(bf16x8, (u32x4{ct[2].x, ct[2].y, ct[3].x, ct[3].y})), cc, f32x16{}); pin(a1[1]);
    }
    STAMP(4);
    asm volatile("" : "=v"(gz), "=v"(ge), "=v"(gr), "=v"(go[0]), "=v"(go[1]));
    f32x16 acc[4] = {};
    bf16x8 wf[WR];
    u32 pk = 0;
    int a2[2][4], ac[4], ta, cv;
    auto rd2 = [&](int n) AI {
      const int k = SQ.k[n], i = SQ.i[n];
      if ((k >= KS && k < KS + 4) || (k >= 22 && k < 34)) {
        const int part = k < 10 ? (k - KS) >> 1 : (k < 26 ? (k - 22) >> 1 : 2 + (((k - 26) >> 1) & 1));
        const int q = (k & 1) + ((k < 10 || (k >= 26 && k < 30)) ? 0 : 2);
        wf[n % WR] = ld128(ac[q] + part * 16384 + i * 4096);
      } else {
        const int p = k < 4 ? k >> 1 : (k < 10 ? 2 : (k < 22 ? (k - 4) >> 1 : (k - 16) >> 1));
        const int sl = W2S[p];
        wf[n % WR] = ldtr(a2[sl >= 12][i] + (sl >= 12 ? sl - 12 : sl) * 8192 + (k & 1) * 4096, 2048);
      }
    };
    u32x4 xo, xi[6];
    u32 xv, xc[3];
    auto event = [&](auto Ee) AI {
      constexpr int idx = decltype(Ee)::v, kind = EV.e[idx].kind, t = EV.e[idx].arg / 3, a = EV.e[idx].arg % 3;
      if constexpr (kind == E_ST) {
        constexpr int k = t < 2 ? 2 * t : (t == 2 ? KT : 42);
        const int off = t == 3 ? T(TB_OFF) : 0;
        if constexpr (a < 2) stg4<a * 1024>(hf[k + a], l16 | off, rw, T(TB_ST + t));
        else stg1(cso[t & 1], l4 | off, rw, T(TB_ST + (t == 1 ? 0 : t)));
      } else if constexpr (kind == E_LD) {
        constexpr int u = EV.e[idx].arg >> 4, e = EV.e[idx].arg & 15, r = u < 6 ? u : 3 * u - 12 + e / 3;
        if constexpr (u < 6 ? e == 4 : e % 3 == 2) ldg1(hc[r], l4, rw, T(TB_LD + r));
        else if constexpr (u < 6) ldg4o<(e >> 1) * REC + (e & 1) * 1024>(hf[gk(u) + e], l16, rw, T(TB_LD + r));
        else ldg4o<e % 3 * 1024>(hf[gk(u) + e / 3 * 2 + e % 3], l16, rw, T(TB_LD + r));
      } else if constexpr (kind == E_XS) {
        if constexpr (a < 2) stg4<a * 1024>(xo, l16, rw, T(TB_XS + t - 1));
        else stg1(xv, l4, rw, T(TB_XS + t - 1));
      } else {
        if constexpr (a < 2) ldg4o<a * 1024>(xi[2 * t + a], l16, rw, T(TB_XI) + t * REC);
        else ldg1(xc[t], l4, rw, T(TB_XI) + t * REC);
      }
    };
    auto fill = [&](auto Gg) AI {
      constexpr int G = decltype(Gg)::v;
      if constexpr (EA && G < 64) {
        sfor<evfirst(G), evfirst(G + 1)>(event);
        SB;
      }
      constexpr int o = G >= GB0, g0 = o ? GB0 : GA0, g1 = o ? GB1 : GA1, v = gstep(G, g0, g1);
      if constexpr (v >= 0) gelu(o ? a1 : a0, IC<v>{}, IC<(gst(v, g0, g1) == gend(v, g0, g1) ? 0 : 1 + G - gst(v, g0, g1))>{}, IC<o>{});
      if constexpr (!EA) sfor<evfirst(G), evfirst(G + 1)>(event);
    };
    sfor<32, 64>([&](auto Nn) AI {
      constexpr int n = decltype(Nn)::v, m = n - 32;
      SB;
      a1[0] = MFMA32(fw[n % 3][0], xr[m], a1[0]); pin(a1[0]);
      SB;
      if (n + 2 < 64) rdw(n + 2, 1);
      if (2 * m + PD >= 64) rd2(2 * m + PD - 64);
      SB;
      fill(IC<2 * m>{});
      if (2 * m + PD == 63) chkv<C_LA, 4>(sv, pk);
      SB;
      a1[1] = MFMA32(fw[n % 3][1], xr[m], a1[1]); pin(a1[1]);
      SB;
      if (n + 2 < 64) rdw(n + 2, 0);
      if (2 * m + 1 + PD >= 64) rd2(2 * m + 1 + PD - 64);
      SB;
      if (m == 3) {
        waitv<0>();
        sig<C_U1>(sv);
      }
      fill(IC<2 * m + 1>{});
      if (m == 13) chkv<C_U1, 8>(sv, pk);
      if (2 * m + 1 + PD == 63) chkv<C_LA, 4>(sv, pk);
      if (m == 10) cv = opq(MI_CNT);
      if (m == 11) pk = peek<C_U1>(cv);
      if (m == 13) sig<C_P1>(sv);
      if (m == 29) sig<C_P2>(sv);
      if (m == 21 && CE == 1) sig<C_P5>(sv);
      if (m == 26) pk = peek<C_LA>(cv);
      if (m == 26) {
        const int a = (4 * h + tq) * 256 + (tq << 6) + (gi & 1) * 32 + tp * 8;
#pragma unroll
        for (int i = 0; i < 4; ++i) a2[0][i] = opq(a ^ (i << 6));
      }
      if (m == 27) {
#pragma unroll
        for (int i = 0; i < 4; ++i) a2[1][i] = opq(a2[0][i] + 98304);
      }
      if (m == 30) {
#pragma unroll
        for (int i = 0; i < 4; ++i) ac[i] = opq(ax ^ (i * 32) ^ T(TB_AX));
      }
      if (m == 31) ta = opq(trb + (8 * h + tq) * 72 + (16 * (gi & 1) + 4 * tp) * 2);
      if (m == 7) STS(1, 5);
      if (m == 15) STS(1, 6);
      if (m == 23) STS(1, 7);
    });
    STS(0, 5); STS(1, 8);
    f32x4 ep[4];
    sfor<64, GE>([&](auto Gg) AI {
      constexpr int G = decltype(Gg)::v, n = G - 64, k = SQ.k[n], i = SQ.i[n], t = chkat(G);
      SB;
      acc[i] = MFMA32(BC(bf16x8, hf[k]), wf[n % WR], acc[i]); pin(acc[i]);
      SB;
      if (n + PD < 176) rd2(n + PD);
      if constexpr (G == gap1(KS) - 4) {
#pragma unroll
        for (int f = 0; f < 4; ++f) hf[KS + f] = BC(u32x4, ldtr(ta + (f >> 1) * 2304 + (f & 1) * 16 * 72, 4 * 72));
      }
      if constexpr (t >= 0) {
        if constexpr (gw(t) >= 0 && G == gchk(t) + gn(t) - 3) pk = peek<gw(t)>(cv);
      }
      SB;
      if constexpr (EA) {
        sfor<evfirst(G), evfirst(G + 1)>(event);
        SB;
      }
      if constexpr (t >= 0) {
        constexpr int z = G - gchk(t), r = t < 7 ? t : 9, nf = gn(t);
        u32x4* f = hf + gk(t);
        if constexpr (z == 0) {
          asm volatile("s_waitcnt vmcnt(%8)\n\tv_add3_u32 %0, %2, %3, %4\n\tv_add3_u32 %1, %5, %6, %7"
                       : "=&v"(c1), "=&v"(c2)
                       : "v"(f[0].x), "v"(f[0].y), "v"(f[0].z), "v"(f[0].w), "v"(f[nf - 1].w),
                         "s"(tlo * (t < 6 ? 1 : 9 - t) - (gw(t) >= 0 ? 4u << 16 : 0u)), "n"(evfirst(G + EA) - evfind(E_LD, t * 16 + gl(t) - 1) - 1)
                       : "memory");
        } else if constexpr (z < nf - 1) {
          asm volatile("v_add3_u32 %0, %0, %2, %3\n\tv_add3_u32 %1, %1, %4, %5" : "+v"(c1), "+v"(c2) : "v"(f[z].x), "v"(f[z].y), "v"(f[z].z), "v"(f[z].w));
        } else if constexpr (z < nf) {
          asm volatile("v_add3_u32 %0, %0, %2, %3\n\tv_add3_u32 %1, %1, %4, %0" : "+v"(c1), "+v"(c2) : "v"(f[z].x), "v"(f[z].y), "v"(f[z].z));
          if constexpr (t < 6) pin(hc[r]);
#pragma unroll
          for (int e = 0; e < nf; ++e) pin(f[e]);
        } else if constexpr (z < gs(t) - 1) {
          if constexpr (t == 6) asm volatile("v_add3_u32 %0, %0, %1, %2" : "+v"(hc[r]) : "v"(hc[r + 1]), "v"(hc[r + 2]));
          if constexpr (t == 7) asm volatile("v_add_u32 %0, %0, %1" : "+v"(hc[r]) : "v"(hc[r + 1]));
          if constexpr (gw(t) >= 0) asm volatile("v_lshl_add_u32 %0, %1, 16, %0" : "+v"(c2) : "v"(pk));
        } else if (__builtin_expect(__builtin_amdgcn_ballot_w64(c2 != hc[r]) != 0, 0)) {
          if constexpr (gw(t) >= 0) {
            c2 += (4u - pk) << 16;
            lwait<gw(t), 4>(sv);
            RETRY(25 + 2 * t);
          }
          if (gw(t) < 0 || __builtin_amdgcn_ballot_w64(c2 != hc[r]) != 0) {
            RETRY(9 + 2 * t);
            if constexpr (t < 6) refetch2(f, l16, rw, T(TB_LD + r), tlo);
            else
#pragma unroll
              for (int e = 0; e < nf / 2; ++e) refetch(f[2 * e], f[2 * e + 1], l16, rw, T(TB_LD + r + e), tlo);
          }
        }
      }
      sfor<1, 4>([&](auto Cc) AI {
        constexpr int c = decltype(Cc)::v, u = G - gpack(c);
        if constexpr (c >= FPK) {
          if constexpr (u == 0 || u == 1) {
#pragma unroll
            for (int e = 0; e < 4; ++e) xo[e] = pk2(acc[c][8 * u + 2 * e], acc[c][8 * u + 2 * e + 1]);
            xv = add3(xo.z, xo.w, add3(xo.x, xo.y, u == 0 ? tlo : xv));
            pin(xo, xv);
          }
        } else if constexpr (u >= 0 && u < 4) {
          u32 p0 = pk2(acc[c][4 * u], acc[c][4 * u + 1]), p1 = pk2(acc[c][4 * u + 2], acc[c][4 * u + 3]);
          xv = add3(p0, p1, u == 0 ? tlo : xv);
          pin(p0, p1, xv);
          xo[(u & 1) * 2] = p0;
          xo[(u & 1) * 2 + 1] = p1;
        }
      });
      fill(Gg);
      if constexpr (G == gap1(KS) - 7) pk = peek<C_PAIR>(opq(cv + rg * 16));
      if constexpr (G == gap1(KS) - 5) chkv<C_PAIR, 1>(svp, pk);
      if constexpr (G == gapof(13, 3) - PD) sig<C_P3>(sv);
      if constexpr (G == gapof(5, 3) - PD) sig<C_P4>(sv);
      if constexpr (G >= GE - 4) ep[G - GE + 4] = LDS(f32x4, trb + (G - GE + 4) * 1024 + l16);
      if constexpr (G >= S7G && G < S7G + 10 * S7D && (G - S7G) % S7D == 0) STS(7, (5 + (G - S7G) / S7D));
      if constexpr (G == gapof(3, 3)) STS(1, 9);
      if constexpr (G == gapof(5, 3)) STS(1, 10);
      if constexpr (G == gapof(7, 3)) STS(1, 11);
      if constexpr (G == gapof(9, 3)) { STS(1, 12); STS(0, 6); }
      if constexpr (G == 64 + 63) STS(0, 7);
      if constexpr (G == 64 + 87) STS(0, 8);
      if constexpr (G == gapof(43, 1)) STS(0, 9);
      if constexpr (G == gapof(43, 2)) STS(0, 10);
      if constexpr (G == gapof(43, 3)) { STS(0, 11); STS(6, 5); }
    });
    SB;
    STS(0, 12); STS(6, 6);
    const int xin = T(TB_XI);
    waitv<EV.n - evfind(E_XS, 11) - 1>();
    STS(0, 13); STS(6, 7);
    ldg4o<0>(xi[4], l16, rw, xin + 2 * REC);
    ldg4o<1024>(xi[5], l16, rw, xin + 2 * REC);
    ldg1(xc[2], l4, rw, xin + 2 * REC);
    f32x2 o[8];
#pragma unroll
    for (int e = 0; e < 8; ++e) o[e] = f32x2{acc[0][2 * e], acc[0][2 * e + 1]} + f32x2{ep[e >> 1][(2 * e) & 3], ep[e >> 1][(2 * e + 1) & 3]};
    sfor<0, 3>([&](auto Ii) AI {
      constexpr int i = decltype(Ii)::v;
      wait3<(i < 2 ? EV.n + 2 - evfind(E_XL, i % 2 * 3 + 2) : 0)>(xi[2 * i], xi[2 * i + 1], xc[i]);
      STS(6, (8 + 2 * i));
      if (__builtin_amdgcn_ballot_w64(csum(xi[2 * i], xi[2 * i + 1], tlo) != xc[i]) != 0) {
        RETRY(50 + 2 * i);
        refetch(xi[2 * i], xi[2 * i + 1], l16, rw, xin + i * REC, tlo);
      }
#pragma unroll
      for (int e = 0; e < 8; ++e) {
        const u32 u = xi[2 * i + (e >> 2)][e & 3];
        o[e] += f32x2{bf2f(u), bfh(u)};
      }
#pragma unroll
      for (int e = 0; e < 8; ++e) pin(o[e]);
      STS(6, (9 + 2 * i));
    });
    STS(0, 14);
    {
      const int oa = opq(h * 8192 + l31 * 4), ob = opq(oa + 4096);
      sfor<0, 16>([&](auto Ee) AI {
        constexpr int e = decltype(Ee)::v;
        stf<(e & 1) * 2048>(o[e >> 1][e & 1], (e & 2) ? +ob : +oa, q4<0>(ke), T(TB_OUT) + (e >> 2) * 16384);
      });
    }
    asm volatile(".Lrole_z:");
    if (T(TB_EP)) stg2<0>(u32x2{tlo, tlo}, lane == 0 ? 0 : OOB, rw);
    done();
  }
}

#define WP(n) const void *m##n, u32 sz##n, u32 fl##n, void *ws##n, u32 ps##n, u32 ca##n, u32 lw##n, u32 bf##n, u32 sh##n, float k0##n, float k1##n, u32 st##n, u32 sw##n, u32 xo##n
#define RD(n) const void *rp##n, u32 rs##n, u32 rf##n
__global__ __launch_bounds__(512, 2) void fused_ssm_ffn(WP(0), WP(1), WP(2), WP(3), WP(4), WP(5), WP(6), WP(7), const void* x, const void* w1,
                                                         const void* bre, const void* bim, const void* lre, const void* lim, const void* ldt,
                                                         const void* b1, const void* dsk, const void* b2, const void* t0, const void* t1,
                                                         const void* t2, const void* t3, RD(out), RD(ws), RD(w1), RD(w2), RD(cre), RD(cim)) {
  Ctx cx;
  cx.stv = 0;
  STAMP(0);
  const u64 ka = (u64)__builtin_amdgcn_kernarg_segment_ptr();
  const int tid = threadIdx.x;
  int w64, b = blockIdx.x;
  u32 j0, j1;
  u32x8 k0;
  asm volatile("s_load_dwordx8 %0, %4, 512\n\tv_readfirstlane_b32 %3, %5\n\ts_load_dword %1, %4, 576\n\ts_nop 2\n\ts_load_dword %2, %4, %3"
               : "=&s"(k0), "=&s"(j0), "=&s"(j1), "=&s"(w64)
               : "s"(ka), "v"(tid)
               : "memory");
  b = opqs(b);
  u64 tab = (u64)TAB - 2560 + (u32)(b * 1024);
  asm volatile("" : "+s"(tab));
  {
    u64 pc;
    int ra, rz;
    asm volatile("s_getpc_b64 %0\n.Lpc0:\n\ts_mov_b32 %1, .Lrole_b-.Lpc0\n\ts_mov_b32 %2, .Lrole_z-.Lpc0" : "=s"(pc), "=s"(ra), "=s"(rz));
    const int s2 = opqs((b & ~7) * 256 - 48 * 128);
    const int o = min((w64 < 192 ? ra : (w64 < 384 ? -20 * 128 : s2)) + (tid & ~7) * 16, rz);
    asm volatile("s_mov_b32 m0, %2\n\ts_nop 0\n\tglobal_load_lds_dword %0, %1" ::"v"(o), "s"(w64 == 320 ? tab : pc), "n"(MI_JUNK) : "memory");
  }
  STN(12);
  b = opqs(b);
  const int r = (b & 4) * 2 + (b >> 5), s = 4 * (b & 3) + ((b >> 3) & 3), g7 = (b & 3) * 7;
  int ox = (r * 16 + s) * 8192 + tid * 16;
  int ow = (tid >> 3) * 3584 + (tid & 7) * 16 + opqs(((b >> 3) & 7) * 64 * 3584 + (g7 + min(r >> 1, 6)) * 128);
  int ob = (s * 16 + r) * 2048 + (tid & 255) * 8;
  pin(ox, ow, ob);
  SB;
  STN(13);
  u64 pb;
  u32x8 k1;
  u32x4 k2;
  asm volatile("s_cmpk_gt_u32 %[w], 255\n\ts_waitcnt lgkmcnt(0)\n\tglobal_load_dwordx4 %[ax], %[ox], %[x]\n\ts_cselect_b64 %[pb], %[bi], %[br]\n\t"
               "global_load_dwordx4 %[aw], %[ow], %[w1]\n\ts_load_dwordx16 %[kw], %[ka], %[w]\n\tglobal_load_dwordx2 %[ab], %[ob], %[pb]\n\t"
               "s_cbranch_scc1 1f\n\ts_load_dwordx8 %[k1], %[ka], 544\n\ts_load_dwordx4 %[k2], %[ka], 576\n1:"
               : [ax] "=&v"(cx.ax_), [aw] "=&v"(cx.aw_), [ab] "=&v"(cx.ab_), [pb] "=&s"(pb), [kw] "=&s"(cx.kw), [k1] "=&s"(k1), [k2] "=&s"(k2)
               : [w] "s"(w64), [ox] "v"(ox), [ow] "v"(ow), [ob] "v"(ob), [x] "s"(kp(k0, 0)), [w1] "s"(kp(k0, 1)), [br] "s"(kp(k0, 2)),
                 [bi] "s"(kp(k0, 3)), [ka] "s"(ka), "s"(j0), "s"(j1)
               : "memory", "scc");
  STN(14);
  b = opqs(b);
  int ol, oe, od, rl, rl4, pos, pk, cj, g14;
  {
    const int g = b & 3, j4 = (b >> 1) & 12;
    ol = s * 64 + (tid & 31) * 2, oe = (j4 + g) * 64 + (tid & 31) * 2;
    od = (tid & 7) * 8 + ((tid >> 3) & 6), rl = (tid >> 3) & 7, rl4 = rl * 4, pos = tid & 7;
    pk = r + s * 256 + ((g7 + ((0x5320 >> j4) & 15)) << 16), cj = j4 + 0x40010, g14 = g7 * 2;
  }
  pin(ol, oe, od, rl, rl4, pos);
  asm volatile("" : "+s"(tab));
  tab += (u32)(w64 * 2 + 2560);
  int o4 = w64 < 256 ? 704 : 624, o2 = (w64 >> 3) & 24;
  asm volatile("" : "+s"(tab), "+s"(o4), "+s"(o2));
  asm volatile("s_waitcnt lgkmcnt(0)" : "+s"(cx.kw), "+s"(k1), "+s"(k2), "+s"(pk), "+s"(cj), "+s"(g14)::"memory");
  STN(11);
  asm volatile("s_load_dwordx16 s[48:63], %1, 0\n\ts_load_dword %0, %2, 0\n\ts_load_dwordx16 s[64:79], %1, 64\n\ts_load_dwordx16 s[80:95], %3, 640\n\t"
               "s_load_dwordx4 s[96:99], %3, %4\n\ts_load_dwordx2 s[46:47], %3, %5 offset:592"
               : "=&s"(cx.tag)
               : "s"(tab), "s"(kp(cx.kw, 2)), "s"(ka), "s"(o4), "s"(o2)
               : "memory", "s46", "s47", "s48", "s49", "s50", "s51", "s52", "s53", "s54", "s55", "s56", "s57", "s58", "s59", "s60", "s61", "s62", "s63", "s64", "s65", "s66", "s67", "s68", "s69", "s70", "s71", "s72", "s73", "s74", "s75", "s76", "s77", "s78", "s79", "s80", "s81", "s82", "s83", "s84", "s85", "s86", "s87", "s88", "s89", "s90", "s91", "s92", "s93", "s94", "s95", "s96", "s97", "s98", "s99");
  asm volatile("s_cmpk_gt_u32 %[w], 255\n\ts_cbranch_scc1 1f\n\tglobal_load_ushort %[lg], %[ol], %[p2]\n\t"
               "s_bfe_u32 %[c], %[sh], %[cj]\n\tglobal_load_ushort %[lr], %[ol], %[p0]\n\ts_add_u32 %[c], %[c], %[g]\n\t"
               "global_load_ushort %[li], %[ol], %[p1]\n\tv_lshl_add_u32 %[od], %[c], 6, %[od]\n\tglobal_load_ushort %[dv], %[oe], %[p4]\n\t"
               "global_load_ushort %[bv], %[oe], %[p5]\n\tglobal_load_ushort %[b1], %[od], %[p3]\n1:"
               : [c] "=&s"(j0), [lg] "=&v"(cx.pc), [lr] "=&v"(cx.pa), [li] "=&v"(cx.pb), [dv] "=&v"(cx.dv), [bv] "=&v"(cx.bv),
                 [b1] "=&v"(cx.pd), [od] "+v"(od)
               : [w] "s"(w64), [sh] "s"(cx.kw[10]), [cj] "s"(cj), [g] "s"(g14), [ol] "v"(ol), [oe] "v"(oe), [p0] "s"(kp(k1, 0)),
                 [p1] "s"(kp(k1, 1)), [p2] "s"(kp(k1, 2)), [p3] "s"(kp(k1, 3)), [p4] "s"(kp(k2, 0)), [p5] "s"(kp(k2, 1))
               : "memory", "scc");
  cx.vm[0] = __umul24(rl, cx.kw[13]) + ((pos ^ __builtin_amdgcn_ubfe(cx.kw[14], rl4, 3)) << 4);
  cx.vm[1] = (cx.vm[0] ^ cx.kw[15]) + cx.kw[6];
  cx.vm[2] = cx.vm[0] + 2 * cx.kw[6];
  cx.vm[3] = cx.vm[1] + 2 * cx.kw[6];
  asm volatile("s_bfe_u32 %0, %1, %2\n\ts_lshl_b32 %0, %0, %3\n\ts_sub_u32 %0, %0, %4"
               : "=&s"(cx.mso)
               : "s"(pk), "s"(cx.kw[9]), "s"(cx.kw[10]), "s"(cx.kw[7])
               : "scc");
  STN(10);
  asm volatile("s_barrier" ::: "memory");
  PIECE(0, 0); PIECE(0, 1); PIECE(0, 2); PIECE(0, 3);
  STN(5);
  const int w = w64 >> 6;
  __builtin_assume(w >= 0 && w < 8);
  if (w >= 4) role<false>(w, tid, cx);
  else role<true>(w, tid, cx);
}

extern "C" void kernel_launch(void* const* d_in, const int* in_sizes, int n_in, void* d_out, int out_size, void* d_ws, size_t ws_size,
                              hipStream_t stream) {
  (void)hipFuncSetAttribute((const void*)fused_ssm_ffn, hipFuncAttributeMaxDynamicSharedMemorySize, LDS_BYTES);
  const char *x = (const char*)d_in[0], *bre = (const char*)d_in[3], *bim = (const char*)d_in[4], *w1 = (const char*)d_in[10];
  constexpr u32 FL = 0x00020000u;
#define WX(n, tl, k0, k1) x + n * 32768, 2048u * 1024u - n * 32768u, FL, d_ws, 7168u, 128u, n * 4096u, 0x80000u, tl << 16 | 17u, k0, k1, 1024u, 0x33221100u, 64u
#define WB_(n, p) p, 512u * 1024u, FL, d_ws, 7168u, 128u, WB + n * 4096u, 0x80008u, 15u, 0.f, 0.f, 1024u, 0x33221100u, 64u
#define WW(n) w1 + (n - 2) * 32 * 3584, (512u - (n - 2) * 32u) * 3584u, FL, d_ws, 8u * 3584u - 1024u, 64u * 3584u, WB + n * 4096u, 0x80010u, 7u, 0.f, 0.f, 3584u, 0x44004400u, 0u
  fused_ssm_ffn<<<256, 512, LDS_BYTES, stream>>>(WX(0, 0xA640u, 1.f, 2.f), WX(1, 0xB751u, 4.f, 8.f), WX(2, 0xC832u, 32.f, 64.f),
                                                 WX(3, 0xD923u, 96.f, 256.f), WB_(0, bre), WB_(1, bim), WW(2), WW(3), x, w1, bre, bim, d_in[1], d_in[2],
                                                 d_in[7], d_in[11], d_in[8], d_in[13], d_in[12], d_in[12], d_in[5], d_in[6], d_out, 2048u * 2048u,
                                                 FL, d_ws, WS_END, FL, w1, 512u * 3584u, FL, d_in[12], 1792u * 1024u, FL, d_in[5],
                                                 512u * 1024u, FL, d_in[6], 512u * 1024u, FL);
}
```
